```python
import torch

def _softmax(x, axis=-1):
    return torch.softmax(x, dim=axis)
def _gelu(x):
    return torch.nn.functional.gelu(x, approximate="tanh")
def _silu(x):
    return torch.nn.functional.silu(x)
def _sigmoid(x):
    return torch.sigmoid(x)
def _layernorm(x, axis=-1, eps=1e-5):
    m = x.mean(dim=axis, keepdim=True)
    v = x.var(dim=axis, keepdim=True, unbiased=False)
    return (x - m) / torch.sqrt(v + eps)
def _scatter_add(tgt, idx, src, axis=0):
    return tgt.clone().index_add_(axis, idx, src)
def _segment_sum(src, seg, n):
    out = torch.zeros((n,) + src.shape[1:], dtype=src.dtype)
    return out.index_add_(0, seg, src)
def _pad(x, pw):
    flat = tuple(v for pair in reversed(pw) for v in pair)
    return torch.nn.functional.pad(x, flat)

kernel_name = 'gated_mix__gqa_block_cached__glu_gated_swiglu__decode'

def setup_inputs():

    _w = (lambda a: (lambda t: t.to(torch.bfloat16) if t.is_floating_point() else t)(torch.as_tensor(a)))
    def _A_setup():
        B, Sq, D = 2, 1, 448
        Hq, Hkv, Sk, Dh = 16, 8, 32768, 256
        assert Hq % Hkv == 0, (Hq, Hkv)
        return {
            "x": _w((lambda *s: torch.randn(*s, dtype=torch.bfloat16) * 0.05)(B, Sq, D)),
            "k_cache": _w((lambda *s: torch.randn(*s, dtype=torch.bfloat16) * 3.2)(B, Hkv, Sk, Dh)),
            "v_cache": _w((lambda *s: torch.randn(*s, dtype=torch.bfloat16) * 0.05)(B, Hkv, Sk, Dh)),
            "wq": _w((lambda *s: torch.randn(*s, dtype=torch.bfloat16) * 3.2)(D, Hq * Dh) * (D ** -0.5)),
            "wo": _w((lambda *s: torch.randn(*s, dtype=torch.bfloat16) * 0.05)(Hq * Dh, D) * ((Hq * Dh) ** -0.5)),
        }
    _A_in = _A_setup()

    def _B_setup():
        B, S, D, F = 2, 1, 448, 1792
        return {
            "x": _w((lambda *s: torch.randn(*s, dtype=torch.bfloat16) * 0.05)(B, S, D)),
            "w1": _w((lambda *s: torch.randn(*s, dtype=torch.bfloat16) * 0.1)(D, F) * (2.0 / D) ** 0.5),
            "w3": _w((lambda *s: torch.randn(*s, dtype=torch.bfloat16) * 0.1)(D, F) * (2.0 / D) ** 0.5),
            "w2": _w((lambda *s: torch.randn(*s, dtype=torch.bfloat16) * 0.1)(F, D) * (2.0 / F) ** 0.5),
        }
    _B_in = _B_setup()

    return {
        "A_x": _A_in["x"],
        "A_k_cache": _A_in["k_cache"],
        "A_v_cache": _A_in["v_cache"],
        "A_wq": _A_in["wq"],
        "A_wo": _A_in["wo"],
        "B_x": _B_in["x"],
        "B_w1": _B_in["w1"],
        "B_w3": _B_in["w3"],
        "B_w2": _B_in["w2"],
        "gate_w": _w((lambda *s: torch.randn(*s, dtype=torch.bfloat16) * 0.05)(448) * 16.0),
    }

def reference(A_x, A_k_cache, A_v_cache, A_wq, A_wo, B_x, B_w1, B_w3, B_w2, gate_w):
    def _A_ref(x, k_cache, v_cache, wq, wo):
        B, Sq, D = x.shape
        Hq, Dh = 16, 256
        Hkv = k_cache.shape[1]
        rep = Hq // Hkv
        q = (lambda x, *d: x.permute(*d))((x @ wq).reshape(B, Sq, Hq, Dh), 0, 2, 1, 3)
        k = (lambda x, n, axis: torch.repeat_interleave(x, n, dim=axis))(k_cache, rep, axis=1)
        v = (lambda x, n, axis: torch.repeat_interleave(x, n, dim=axis))(v_cache, rep, axis=1)
        scores = torch.einsum("bhqd,bhkd->bhqk", q, k) * (Dh ** -0.5)
        p = _softmax(scores, axis=-1)
        o = torch.einsum("bhqk,bhkd->bhqd", p, v)
        return (lambda x, *d: x.permute(*d))(o, 0, 2, 1, 3).reshape(B, Sq, Hq * Dh) @ wo

    def _B_ref(x, w1, w3, w2):
        gate = torch.einsum("bsd,df->bsf", x, w1)
        up = torch.einsum("bsd,df->bsf", x, w3)
        h = _silu(gate) * up
        out = torch.einsum("bsf,fd->bsd", h, w2)
        return out

    x = A_x
    g = _sigmoid((x * gate_w).sum(-1, keepdims=True))
    a = _A_ref(x, A_k_cache, A_v_cache, A_wq, A_wo)
    b = _B_ref(x, B_w1, B_w3, B_w2)
    return g * a + (1 - g) * b
```

```cpp
#include <hip/hip_runtime.h>
#include <hip/hip_bf16.h>
#include <stdint.h>

namespace {

#define GAS __attribute__((address_space(1)))
typedef unsigned v4u __attribute__((ext_vector_type(4)));
typedef unsigned v2u __attribute__((ext_vector_type(2)));
typedef float f32x4 __attribute__((ext_vector_type(4)));
typedef __bf16 bf16x8 __attribute__((ext_vector_type(8)));
typedef __bf16 bf16x2 __attribute__((ext_vector_type(2)));
typedef unsigned long long u64;
typedef __amdgpu_buffer_rsrc_t rsrc_t;

constexpr int DM = 448, SK = 32768, FF = 1792, QN = 4096;
constexpr int NWG = 256, NTHR = 512;
#define WIN 4
constexpr int TILES_PER_WAVE = 16;
constexpr int NRED = 56;
constexpr int AUX_NT = 2, AUX_SC1 = 16;
constexpr unsigned SENT = 0xFFFFFFFFu;
constexpr unsigned SPIN_CAP = 1u << 18;
constexpr float SCL = 0.0625f * 1.4426950408889634f;

constexpr unsigned WS_EPOCH = 0;
constexpr unsigned WS_QG = 4096;
constexpr unsigned WS_REC = WS_QG + 2 * QN * 8;
constexpr unsigned REC_O = 0, REC_Y = 2048, REC_LSE = 5632, REC_USED16 = 353, REC_STRIDE = 5760;
constexpr unsigned WS_END = WS_REC + NWG * REC_STRIDE;
#define STAMP(i) do { } while (0)

__device__ __forceinline__ rsrc_t mk_rsrc(const void* p, unsigned bytes) { return __builtin_amdgcn_make_buffer_rsrc((void*)p, 0, (int)bytes, 0x00020000); }
__device__ __forceinline__ float u2f(unsigned u) { return __builtin_bit_cast(float, u); }
__device__ __forceinline__ unsigned f2u(float f) { return __builtin_bit_cast(unsigned, f); }
__device__ __forceinline__ float bf_lo(unsigned u) { return __builtin_bit_cast(float, u << 16); }
__device__ __forceinline__ float bf_hi(unsigned u) { return __builtin_bit_cast(float, u & 0xffff0000u); }
__device__ __forceinline__ unsigned cvt_pk(float lo, float hi) {
  bf16x2 r = {(__bf16)lo, (__bf16)hi};
  return __builtin_bit_cast(unsigned, r);
}
__device__ __forceinline__ float bf_round(float x) { return bf_lo(cvt_pk(x, x) & 0xffffu); }
__device__ __forceinline__ float dot2(unsigned a, unsigned b, float c) {
  return __builtin_amdgcn_fdot2_f32_bf16(__builtin_bit_cast(bf16x2, a), __builtin_bit_cast(bf16x2, b), c, false);
}
template <int CTRL> __device__ __forceinline__ float dpp(float x) {
  return __builtin_bit_cast(float, __builtin_amdgcn_mov_dpp(__builtin_bit_cast(int, x), CTRL, 0xf, 0xf, true));
}
constexpr int DPP_XOR1 = 0xB1, DPP_XOR2 = 0x4E, DPP_ROR4 = 0x124, DPP_ROR8 = 0x128, DPP_SHL4 = 0x104, DPP_SHR4 = 0x114;
__device__ __forceinline__ float sw16_add(float a, float b) {
  auto s = __builtin_amdgcn_permlane16_swap(f2u(a), f2u(b), false, false);
  return u2f(s[0]) + u2f(s[1]);
}
__device__ __forceinline__ float sw32_add(float a, float b) {
  auto s = __builtin_amdgcn_permlane32_swap(f2u(a), f2u(b), false, false);
  return u2f(s[0]) + u2f(s[1]);
}
__device__ __forceinline__ float rows_max(float x) {
  auto s = __builtin_amdgcn_permlane16_swap(f2u(x), f2u(x), false, false);
  x = fmaxf(u2f(s[0]), u2f(s[1]));
  auto t = __builtin_amdgcn_permlane32_swap(f2u(x), f2u(x), false, false);
  return fmaxf(u2f(t[0]), u2f(t[1]));
}
__device__ __forceinline__ float rows_sum(float x) { x = sw16_add(x, x); return sw32_add(x, x); }
__device__ __forceinline__ float s4_sum(float x) { x += dpp<DPP_ROR8>(x); x += dpp<DPP_ROR4>(x); return rows_sum(x); }
__device__ __forceinline__ float s4_max(float x) { x = fmaxf(x, dpp<DPP_ROR8>(x)); x = fmaxf(x, dpp<DPP_ROR4>(x)); return rows_max(x); }
__device__ __forceinline__ f32x4 mfma(v4u a, v4u b, f32x4 c) {
  return __builtin_amdgcn_mfma_f32_16x16x32_bf16(__builtin_bit_cast(bf16x8, a), __builtin_bit_cast(bf16x8, b), c, 0, 0, 0);
}
__device__ __forceinline__ float rdlane(float x, int l) { return __builtin_bit_cast(float, __builtin_amdgcn_readlane(__builtin_bit_cast(int, x), l)); }
__device__ __forceinline__ unsigned nosent(float x) { unsigned u = __builtin_bit_cast(unsigned, x); return u < SENT ? u : SENT - 1u; }
#define LDS_BARRIER() do { asm volatile("s_waitcnt lgkmcnt(0)" ::: "memory"); __builtin_amdgcn_s_barrier(); asm volatile("" ::: "memory"); } while (0)
#define SCHED_FENCE() __builtin_amdgcn_sched_barrier(0)

__device__ __forceinline__ float proj_reduce(v4u ra, v4u rb, unsigned x0, unsigned x1, int lane) {
  float v[8];
#pragma unroll
  for (int c = 0; c < 8; ++c) {
    const unsigned pk = __builtin_amdgcn_perm(rb[c >> 1], ra[c >> 1], (c & 1) ? 0x07060302u : 0x05040100u);
    v[c] = sw32_add(dot2(pk, x0, 0.f), dot2(pk, x1, 0.f));
  }
#pragma unroll
  for (int c = 0; c < 4; ++c) v[c] = sw16_add(v[c], v[c + 4]);
#pragma unroll
  for (int c = 0; c < 2; ++c) { const float lo = v[c] + dpp<DPP_ROR8>(v[c]), hi = v[c + 2] + dpp<DPP_ROR8>(v[c + 2]); v[c] = (lane & 8) ? hi : lo; }
  const float lo = v[0] + dpp<DPP_SHL4>(v[0]), hi = v[1] + dpp<DPP_SHR4>(v[1]);
  float r = (lane & 4) ? hi : lo;
  return r + dpp<DPP_XOR2>(r);
}

struct Attn {
  f32x4 acc[8];
  float m, mc, l;
};

__device__ __forceinline__ v4u ldk(rsrc_t r, int voff, int soff, int i) { return __builtin_amdgcn_raw_buffer_load_b128(r, voff + (i >> 1) * 1024 + (i & 1) * 256, soff, AUX_NT); }
__device__ __forceinline__ v4u ldv(rsrc_t r, int voff, int soff, int i) { return __builtin_amdgcn_raw_buffer_load_b128(r, voff + (i & 3) * 1024, soff + (i >> 2) * 4096, AUX_NT); }

template <int MODE>
__device__ __forceinline__ void tile_step(Attn& st, v4u (&kc)[8], v4u (&vc)[8], v4u (&kn)[8], const v4u (&qf)[2], rsrc_t rk, rsrc_t rv,
                                          int vk, int vv, int so_cur, int so_nxt, int lane) {
  f32x4 s[4];
#pragma unroll
  for (int i = 0; i < 8; ++i) {
    s[i >> 1] = mfma(kc[i], qf[i & 1], (i & 1) ? s[i >> 1] : f32x4{0.f, 0.f, 0.f, 0.f});
    if (MODE == 2) {
      asm volatile("" : "+v"(s[i >> 1]) :: "memory");
      SCHED_FENCE();
      if (i + WIN < 8) kc[i + WIN] = ldk(rk, vk, so_cur, i + WIN); else vc[i + WIN - 8] = ldv(rv, vv, so_cur, i + WIN - 8);
      SCHED_FENCE();
    }
  }
  float sc[4];
#pragma unroll
  for (int a = 0; a < 4; ++a) sc[a] = (dpp<0x00>(s[a][0]) + dpp<0x55>(s[a][1])) + (dpp<0xAA>(s[a][2]) + dpp<0xFF>(s[a][3]));
  const float tmax = rows_max(fmaxf(fmaxf(sc[0], sc[1]), fmaxf(sc[2], sc[3])));
  if (__builtin_amdgcn_ballot_w64(tmax > st.m) != 0ull) {
    const float mn = fmaxf(st.m, tmax), mcn = mn * SCL;
    const float alpha = __builtin_amdgcn_exp2f(st.mc - mcn);
    const float a0 = rdlane(alpha, 0), a1 = rdlane(alpha, 4);
    const float aa = (lane & 16) ? a1 : a0;
#pragma unroll
    for (int d = 0; d < 8; ++d) st.acc[d] *= aa;
    st.l *= alpha; st.m = mn; st.mc = mcn;
  }
  float p[4];
#pragma unroll
  for (int a = 0; a < 4; ++a) p[a] = __builtin_amdgcn_exp2f(fmaf(sc[a], SCL, -st.mc));
  st.l += (p[0] + p[1]) + (p[2] + p[3]);
  v4u pa;
  {
    const unsigned w0 = cvt_pk(p[0], p[1]), w1 = cvt_pk(p[2], p[3]);
    const bool odd = lane & 1;
    auto s0 = __builtin_amdgcn_permlane16_swap(odd ? 0u : w0, odd ? w0 : 0u, false, false);
    auto s1 = __builtin_amdgcn_permlane16_swap(odd ? 0u : w1, odd ? w1 : 0u, false, false);
    pa = v4u{s0[0], s1[0], s0[1], s1[1]};
  }
  if (MODE != 0) {
    asm volatile("" : "+v"(pa) :: "memory");
#pragma unroll
    for (int i = 0; i < 8; ++i) {
      if (MODE == 2 || i + WIN >= 8) {
        SCHED_FENCE();
        asm volatile("s_waitcnt vmcnt(%0)" :: "n"(MODE == 2 ? WIN - 1 : WIN - 1) : "memory");
        if (i + WIN < 8) vc[i + WIN] = ldv(rv, vv, so_cur, i + WIN); else kn[i + WIN - 8] = ldk(rk, vk, so_nxt, i + WIN - 8);
        SCHED_FENCE();
      }
    }
  }
#pragma unroll
  for (int d = 0; d < 8; ++d) {
    v4u vb;
#pragma unroll
    for (int mm = 0; mm < 4; ++mm) vb[mm] = __builtin_amdgcn_perm(vc[2 * mm + 1][d >> 1], vc[2 * mm][d >> 1], (d & 1) ? 0x07060302u : 0x05040100u);
    st.acc[d] = mfma(pa, vb, st.acc[d]);
  }
  if (MODE != 0) {
    asm volatile("" : "+v"(st.acc[0]), "+v"(st.acc[1]), "+v"(st.acc[2]), "+v"(st.acc[3]), "+v"(st.acc[4]), "+v"(st.acc[5]), "+v"(st.acc[6]), "+v"(st.acc[7]) :: "memory");
    SCHED_FENCE();
  }
}

__global__ void __launch_bounds__(NTHR, 2)
fused_decode(const unsigned short* x, const unsigned char* kc_, const unsigned char* vc_,
             const unsigned short* wq, const unsigned short* wo, const unsigned short* w1,
             const unsigned short* w3, const unsigned short* w2, const unsigned short* gw,
             unsigned short* out, unsigned char* ws) {
  __shared__ __attribute__((aligned(16))) float lds[8 * 516 + 2 * 8 * 32 + 256 + 8 * 16];
  float* const l_mrg = lds;
  float* const l_redq = lds + 8 * 516;
  float* const l_redf = l_redq + 8 * 32;
  unsigned short* const l_q = (unsigned short*)(l_redf + 8 * 32);
  float* const l_fin = l_redf + 8 * 32 + 256;

  const int tid = threadIdx.x, lane = tid & 63, wv = __builtin_amdgcn_readfirstlane(tid >> 6);
  const int w = blockIdx.x, pair = w >> 4, b = pair >> 3, kvh = pair & 7, j = w & 15;
  STAMP(0);
  const rsrc_t rws = mk_rsrc(ws, WS_END);
  const rsrc_t rk = mk_rsrc(kc_ + (size_t)pair * SK * 512, SK * 512), rv = mk_rsrc(vc_ + (size_t)pair * SK * 512, SK * 512);
  const rsrc_t rx = mk_rsrc(x, 2 * DM * 2), rwq = mk_rsrc(wq, DM * QN * 2);

  const u64 epoch = __hip_atomic_load((GAS u64*)(ws + WS_EPOCH), __ATOMIC_RELAXED, __HIP_MEMORY_SCOPE_AGENT);
  const unsigned rec = WS_REC + w * REC_STRIDE;
  if (tid < (int)REC_USED16) __builtin_amdgcn_raw_buffer_store_b128(v4u{SENT, SENT, SENT, SENT}, rws, rec + tid * 16, 0, AUX_SC1);
  const int rp = tid >> 1;
  const unsigned x0 = __builtin_amdgcn_raw_buffer_load_b32(rx, rp * 4, 0, 0), x1 = __builtin_amdgcn_raw_buffer_load_b32(rx, DM * 2 + rp * 4, 0, 0);
  const int qcol0 = kvh * 512 + (j & 7) * 64 + (b * 2 + (j >> 3)) * 16;
  const int wqo = rp * 2 * QN * 2 + qcol0 * 2 + (tid & 1) * 16;
  const v4u wqa = __builtin_amdgcn_raw_buffer_load_b128(rwq, wqo, 0, 0), wqb = __builtin_amdgcn_raw_buffer_load_b128(rwq, wqo, QN * 2, 0);
  SCHED_FENCE();
  const int xc = w & 7, xa = w >> 3;
  const bool has_ffn = xc < 4 || xa < 24;
  const int fs = (xc < 4 ? 4 * xc + (xa >> 3) : 16 + 3 * (xc - 4) + (xa >> 3)) * 8 + (xa & 7);
  const unsigned fbytes = has_ffn ? DM * FF * 2 : 0;
  const rsrc_t rw1 = mk_rsrc(w1, fbytes), rw3 = mk_rsrc(w3, fbytes), rw2 = mk_rsrc(w2, (has_ffn && wv < 4) ? FF * DM * 2 : 0);
  v4u fa, fb;
  {
    const int fo = rp * 2 * FF * 2 + fs * 16;
    const int o1 = (tid & 1) ? 0x40000000 : fo, o3 = (tid & 1) ? fo : 0x40000000;
    fa = __builtin_amdgcn_raw_buffer_load_b128(rw1, o1, 0, 0) | __builtin_amdgcn_raw_buffer_load_b128(rw3, o3, 0, 0);
    fb = __builtin_amdgcn_raw_buffer_load_b128(rw1, o1, FF * 2, 0) | __builtin_amdgcn_raw_buffer_load_b128(rw3, o3, FF * 2, 0);
  }
  const int yb = (wv >> 1) & 1;
  const int yco = lane < 56 ? ((wv & 1) * 224 + lane * 4) * 2 : 0x40000000;
  v2u w2r[8];
#pragma unroll
  for (int r = 0; r < 8; ++r) w2r[r] = __builtin_amdgcn_raw_buffer_load_b64(rw2, yco, (fs * 8 + r) * DM * 2, AUX_NT);
  const rsrc_t rg = mk_rsrc(gw, DM * 2), rxb = mk_rsrc(x + yb * DM, DM * 2);
  const v4u gx = __builtin_amdgcn_raw_buffer_load_b128(rxb, lane * 16, 0, 0), gg = __builtin_amdgcn_raw_buffer_load_b128(rg, lane * 16, 0, 0);
  SCHED_FENCE();
  const int krow = (lane & 15) >> 2;
  const int vk = ((krow & 1) * 8 + (krow >> 1)) * 512 + (lane >> 4) * 64 + (lane & 3) * 16, vv = lane * 16;
  const int so0 = (j * 128 + wv) * 8192;
  v4u ka[8], va[8], kb[8], vb[8];
#pragma unroll
  for (int i = 0; i < 8; ++i) ka[i] = ldk(rk, vk, so0, i);
  SCHED_FENCE();
  {
    const float r = proj_reduce(wqa, wqb, x0, x1, lane);
    if (!(lane & 2)) l_redq[wv * 32 + (lane >> 5) * 16 + (lane & 1) * 8 + ((lane >> 2) & 7)] = r;
  }
  STAMP(1);
  LDS_BARRIER();
  const u64 tagw = (epoch + 1ull) << 16;
  if (tid < 32) {
    float s = 0.f;
#pragma unroll
    for (int v = 0; v < 7; ++v) s += l_redq[v * 32 + tid];
    const u64 g = tagw | (u64)(cvt_pk(s, s) & 0xffffu);
    __hip_atomic_store((GAS u64*)(ws + WS_QG) + (tid >> 4) * QN + qcol0 + (tid & 15), g, __ATOMIC_RELAXED, __HIP_MEMORY_SCOPE_AGENT);
  }
  STAMP(2);
  SCHED_FENCE();
#pragma unroll
  for (int i = 0; i < 8; ++i) va[i] = ldv(rv, vv, so0, i);
  SCHED_FENCE();
  const int qgo = WS_QG + (b * QN + kvh * 512 + tid) * 8;
  v2u gq = __builtin_amdgcn_raw_buffer_load_b64(rws, qgo, 0, AUX_SC1);
  SCHED_FENCE();
#pragma unroll
  for (int i = 0; i < 8; ++i) kb[i] = ldk(rk, vk, so0 + 8 * 8192, i);
#pragma unroll
  for (int i = 0; i < 8; ++i) vb[i] = ldv(rv, vv, so0 + 8 * 8192, i);
  SCHED_FENCE();
  {
    const float r = proj_reduce(fa, fb, x0, x1, lane);
    if (!(lane & 2)) l_redf[wv * 32 + (lane >> 5) * 16 + (lane & 1) * 8 + ((lane >> 2) & 7)] = r;
  }
  STAMP(3);
  {
    const unsigned tlo = (unsigned)tagw, thi = (unsigned)(tagw >> 32);
    unsigned spins = 0;
    if (__builtin_amdgcn_ballot_w64(((gq.x ^ tlo) >> 16) != 0u || gq.y != thi) != 0ull) {
#pragma nounroll
      do {
        asm volatile("" ::: "memory");
        gq = __builtin_amdgcn_raw_buffer_load_b64(rws, qgo, 0, AUX_SC1);
      } while (__builtin_amdgcn_ballot_w64(((gq.x ^ tlo) >> 16) != 0u || gq.y != thi) != 0ull && ++spins < SPIN_CAP);
    }
    l_q[tid] = (unsigned short)gq.x;
  }
  STAMP(4);
  LDS_BARRIER();
  v4u qf[2];
  {
    const int qo = ((lane >> 2) & 1) * 256 + (lane >> 4) * 32 + (lane & 3) * 8;
    qf[0] = *(const v4u*)(l_q + qo); qf[1] = *(const v4u*)(l_q + qo + 128);
  }
  float gate = 0.f;
  f32x4 yf = {0.f, 0.f, 0.f, 0.f};
  if (wv < 4) {
    float t = 0.f;
#pragma unroll
    for (int k = 0; k < 4; ++k) t += bf_round(bf_lo(gx[k]) * bf_lo(gg[k])) + bf_round(bf_hi(gx[k]) * bf_hi(gg[k]));
    t += dpp<DPP_XOR1>(t); t += dpp<DPP_XOR2>(t); t = s4_sum(t);
    t = bf_round(t);
    gate = bf_round(1.f / (1.f + __expf(-t)));
    const float omg = bf_round(1.f - gate);
    float ga = 0.f, up = 0.f;
#pragma unroll
    for (int v = 0; v < 7; ++v) { ga += l_redf[v * 32 + yb * 16 + (lane & 7)]; up += l_redf[v * 32 + yb * 16 + 8 + (lane & 7)]; }
    ga = bf_round(ga); up = bf_round(up);
    const float h = bf_round(bf_round(ga / (1.f + __expf(-ga))) * up);
#pragma unroll
    for (int r = 0; r < 8; ++r) {
      const float hr = rdlane(h, r);
      yf[0] = fmaf(hr, bf_lo(w2r[r].x), yf[0]); yf[1] = fmaf(hr, bf_hi(w2r[r].x), yf[1]);
      yf[2] = fmaf(hr, bf_lo(w2r[r].y), yf[2]); yf[3] = fmaf(hr, bf_hi(w2r[r].y), yf[3]);
    }
    yf *= omg;
  }
  asm volatile("" : "+v"(yf), "+v"(gate) :: "memory");
  SCHED_FENCE();

  Attn st;
#pragma unroll
  for (int d = 0; d < 8; ++d) st.acc[d] = f32x4{0.f, 0.f, 0.f, 0.f};
  st.m = -INFINITY; st.mc = -INFINITY; st.l = 0.f;
  tile_step<0>(st, ka, va, ka, qf, rk, rv, vk, vv, 0, 0, lane);
  asm volatile("" : "+v"(st.acc[0]), "+v"(st.acc[1]), "+v"(st.acc[2]), "+v"(st.acc[3]), "+v"(st.acc[4]), "+v"(st.acc[5]), "+v"(st.acc[6]), "+v"(st.acc[7]) :: "memory");
  SCHED_FENCE();
  STAMP(5);
  tile_step<1>(st, kb, vb, ka, qf, rk, rv, vk, vv, 0, so0 + 16 * 8192, lane);
  STAMP(6);
  const rsrc_t rwo = mk_rsrc(wo, wv < 4 ? QN * DM * 2 : 0);
  v2u wor[16];
#pragma unroll
  for (int r = 0; r < 16; ++r) wor[r] = __builtin_amdgcn_raw_buffer_load_b64(rwo, yco, ((kvh * 2 + b) * 256 + j * 16 + r) * DM * 2, AUX_NT);
  SCHED_FENCE();
  for (int n = 2; n < TILES_PER_WAVE; ++n) {
    const int so = so0 + n * 8 * 8192;
    tile_step<2>(st, ka, va, ka, qf, rk, rv, vk, vv, so, n + 1 < TILES_PER_WAVE ? so + 8 * 8192 : SK * 512, lane);
  }
  STAMP(7);

  v2u hello0, hello1;
  const rsrc_t rred = mk_rsrc(ws, w < NRED ? WS_END : 0);
  {
    const int p0 = tid >> 2, p1 = p0 + 128;
    const int c0 = ((p0 >> 4) & 7) * 512 + (p0 & 7) * 64 + ((p0 >> 3) & 1) * 16, c1 = c0 + 32;
    (void)p1;
    hello0 = __builtin_amdgcn_raw_buffer_load_b64(rred, WS_QG + c0 * 8, 0, AUX_SC1);
    hello1 = __builtin_amdgcn_raw_buffer_load_b64(rred, WS_QG + c1 * 8, 0, AUX_SC1);
  }
  {
    const float lsum = rows_sum(st.l);
    float* rec_l = l_mrg + wv * 516;
    const bool hi = lane >= 32;
    const f32x4 o0 = {hi ? st.acc[0][1] : st.acc[0][0], hi ? st.acc[1][1] : st.acc[1][0], hi ? st.acc[2][1] : st.acc[2][0], hi ? st.acc[3][1] : st.acc[3][0]};
    const f32x4 o1 = {hi ? st.acc[4][1] : st.acc[4][0], hi ? st.acc[5][1] : st.acc[5][0], hi ? st.acc[6][1] : st.acc[6][0], hi ? st.acc[7][1] : st.acc[7][0]};
    float* dst = rec_l + ((lane >> 4) & 1) * 256 + (lane >> 5) * 128 + (lane & 15) * 8;
    *(f32x4*)dst = o0; *(f32x4*)(dst + 4) = o1;
    if ((lane & ~4) == 0) { rec_l[512 + (lane >> 2)] = st.mc; rec_l[514 + (lane >> 2)] = lsum; }
  }
  LDS_BARRIER();
  if (wv < 2) {
    float mw[8], M = -INFINITY;
#pragma unroll
    for (int v = 0; v < 8; ++v) { mw[v] = l_mrg[v * 516 + 512 + wv]; M = fmaxf(M, mw[v]); }
    float L = 0.f; f32x4 o = {0.f, 0.f, 0.f, 0.f};
#pragma unroll
    for (int v = 0; v < 8; ++v) {
      const float e = __builtin_amdgcn_exp2f(mw[v] - M);
      L = fmaf(e, l_mrg[v * 516 + 514 + wv], L);
      o += e * *(const f32x4*)(l_mrg + v * 516 + tid * 4);
    }
    const float inv = 1.f / L;
    __builtin_amdgcn_raw_buffer_store_b128(v4u{nosent(o[0] * inv), nosent(o[1] * inv), nosent(o[2] * inv), nosent(o[3] * inv)}, rws, rec + REC_O + tid * 16, 0, AUX_SC1);
    if (lane == 0) __builtin_amdgcn_raw_buffer_store_b32(nosent(M + __builtin_amdgcn_logf(L)), rws, rec + REC_LSE + wv * 4, 0, AUX_SC1);
  }
  STAMP(8);
  if (wv < 4) {
    const unsigned src = WS_REC + ((yb * 8 + kvh) * 16 + (lane >> 2)) * REC_STRIDE;
    const int oo = src + REC_O + (b * 256 + j * 16 + (lane & 3) * 4) * 4, lo = src + REC_LSE + b * 4;
    v4u po; unsigned pl; unsigned spins = 0;
#pragma nounroll
    for (;;) {
      po = __builtin_amdgcn_raw_buffer_load_b128(rws, oo, 0, AUX_SC1);
      pl = __builtin_amdgcn_raw_buffer_load_b32(rws, lo, 0, AUX_SC1);
      const bool bad = po.x == SENT || po.y == SENT || po.z == SENT || po.w == SENT || pl == SENT;
      if (__builtin_amdgcn_ballot_w64(bad) == 0ull || ++spins > SPIN_CAP) break;
      asm volatile("" ::: "memory");
    }
    STAMP(9);
    const float lse = u2f(pl);
    const float M = s4_max(lse), e = __builtin_amdgcn_exp2f(lse - M);
    const float inv = 1.f / s4_sum(e);
    float o[4];
#pragma unroll
    for (int k = 0; k < 4; ++k) o[k] = bf_round(s4_sum(e * u2f(po[k])) * inv);
    f32x4 y = {0.f, 0.f, 0.f, 0.f};
#pragma unroll
    for (int r = 0; r < 16; ++r) {
      const float orr = rdlane(o[r & 3], r >> 2);
      y[0] = fmaf(orr, bf_lo(wor[r].x), y[0]); y[1] = fmaf(orr, bf_hi(wor[r].x), y[1]);
      y[2] = fmaf(orr, bf_lo(wor[r].y), y[2]); y[3] = fmaf(orr, bf_hi(wor[r].y), y[3]);
    }
    y = gate * y + yf;
    if (lane < 56) __builtin_amdgcn_raw_buffer_store_b128(v4u{nosent(y[0]), nosent(y[1]), nosent(y[2]), nosent(y[3])}, rws, rec + REC_Y + yb * DM * 4 + yco * 2, 0, AUX_SC1);
  }
  STAMP(10);
  if (w >= NRED) return;
  {
    const unsigned tlo = (unsigned)tagw, thi = (unsigned)(tagw >> 32);
    const int p0 = tid >> 2;
    const int c0 = ((p0 >> 4) & 7) * 512 + (p0 & 7) * 64 + ((p0 >> 3) & 1) * 16;
    unsigned spins = 0;
    if (__builtin_amdgcn_ballot_w64(((hello0.x ^ tlo) >> 16) != 0u || hello0.y != thi || ((hello1.x ^ tlo) >> 16) != 0u || hello1.y != thi) != 0ull) {
#pragma nounroll
      do {
        asm volatile("" ::: "memory");
        hello0 = __builtin_amdgcn_raw_buffer_load_b64(rws, WS_QG + c0 * 8, 0, AUX_SC1);
        hello1 = __builtin_amdgcn_raw_buffer_load_b64(rws, WS_QG + (c0 + 32) * 8, 0, AUX_SC1);
      } while (__builtin_amdgcn_ballot_w64(((hello0.x ^ tlo) >> 16) != 0u || hello0.y != thi || ((hello1.x ^ tlo) >> 16) != 0u || hello1.y != thi) != 0ull && ++spins < SPIN_CAP);
    }
    asm volatile("" ::: "memory");
    const int yo = WS_REC + p0 * REC_STRIDE + REC_Y + (w * 16 + (tid & 3) * 4) * 4;
    v4u y0, y1;
#pragma nounroll
    for (;;) {
      y0 = __builtin_amdgcn_raw_buffer_load_b128(rws, yo, 0, AUX_SC1);
      y1 = __builtin_amdgcn_raw_buffer_load_b128(rws, yo, 128 * REC_STRIDE, AUX_SC1);
      const bool bad = y0.x == SENT || y0.y == SENT || y0.z == SENT || y0.w == SENT || y1.x == SENT || y1.y == SENT || y1.z == SENT || y1.w == SENT;
      if (__builtin_amdgcn_ballot_w64(bad) == 0ull || ++spins > SPIN_CAP) break;
      asm volatile("" ::: "memory");
    }
    STAMP(11);
    f32x4 y;
#pragma unroll
    for (int k = 0; k < 4; ++k) y[k] = s4_sum(u2f(y0[k]) + u2f(y1[k]));
    if (lane < 4) *(f32x4*)(l_fin + wv * 16 + lane * 4) = y;
    LDS_BARRIER();
    if (tid < 16) {
      float s = 0.f;
#pragma unroll
      for (int v = 0; v < 8; ++v) s += l_fin[v * 16 + tid];
      out[w * 16 + tid] = (unsigned short)(cvt_pk(s, s) & 0xffffu);
    }
    if (w == 0 && tid == 0) __hip_atomic_store((GAS u64*)(ws + WS_EPOCH), epoch + 1ull, __ATOMIC_RELAXED, __HIP_MEMORY_SCOPE_AGENT);
    STAMP(12);
  }
}

}

extern "C" void kernel_launch(void* const* d_in, const int* in_sizes, int n_in, void* d_out, int out_size, void* d_ws, size_t ws_size,
                              hipStream_t stream) {
  (void)in_sizes; (void)n_in; (void)out_size; (void)ws_size;
  fused_decode<<<NWG, NTHR, 0, stream>>>((const unsigned short*)d_in[0], (const unsigned char*)d_in[1], (const unsigned char*)d_in[2],
                                         (const unsigned short*)d_in[3], (const unsigned short*)d_in[4], (const unsigned short*)d_in[6],
                                         (const unsigned short*)d_in[7], (const unsigned short*)d_in[8], (const unsigned short*)d_in[9],
                                         (unsigned short*)d_out, (unsigned char*)d_ws);
}
```

```cpp
#include <hip/hip_runtime.h>
#include <hip/hip_bf16.h>
#include <stdint.h>

namespace {

#define GAS __attribute__((address_space(1)))
#define LAS __attribute__((address_space(3)))
typedef unsigned v4u __attribute__((ext_vector_type(4)));
typedef unsigned v2u __attribute__((ext_vector_type(2)));
typedef float f32x4 __attribute__((ext_vector_type(4)));
typedef __bf16 bf16x8 __attribute__((ext_vector_type(8)));
typedef __bf16 bf16x2 __attribute__((ext_vector_type(2)));
typedef unsigned long long u64;
typedef __amdgpu_buffer_rsrc_t rsrc_t;

constexpr int DM = 448, SK = 32768, FF = 1792, QN = 4096;
constexpr int NWG = 256, NTHR = 512;
constexpr int WIN = 6;
constexpr int NST = 6;
constexpr int NRW = 15;
constexpr int TRIG = 3;
constexpr int POLLP = 17;
constexpr int QR = 8 * (NRW - 2 * NST), QP = 64 * (16 - NRW);
static_assert(WIN >= 4 && WIN <= 7 && NST >= 3 && 2 * NST < NRW && NRW < 16, "knobs");
constexpr int NRED = 56;
constexpr int AUX_NT = 2, AUX_SC1 = 16;
constexpr unsigned SENT = 0xFFFFFFFFu;
constexpr unsigned SPIN_CAP = 1u << 18;
constexpr float SCL = 0.0625f * 1.4426950408889634f;
constexpr int OOB = SK * 512;
constexpr int HALF = 65536;

constexpr unsigned WS_EPOCH = 0;
constexpr unsigned WS_HELLO = 2048;
constexpr unsigned WS_QG = 4096;
constexpr unsigned WS_REC = WS_QG + 2 * QN * 8;
constexpr unsigned REC_O = 0, REC_Y = 2048, REC_STRIDE = 5632;
constexpr unsigned WS_CNT = WS_REC + NWG * REC_STRIDE, CSTR = 768;
constexpr unsigned WS_FX = WS_CNT + 32 * CSTR;
constexpr unsigned WS_END = WS_FX + 224 * 1024;

__device__ __forceinline__ rsrc_t mk_rsrc(const void* p, unsigned bytes) { return __builtin_amdgcn_make_buffer_rsrc((void*)p, 0, (int)bytes, 0x00020000); }
__device__ __forceinline__ float u2f(unsigned u) { return __builtin_bit_cast(float, u); }
__device__ __forceinline__ unsigned f2u(float f) { return __builtin_bit_cast(unsigned, f); }
__device__ __forceinline__ float bf_lo(unsigned u) { return u2f(u << 16); }
__device__ __forceinline__ float bf_hi(unsigned u) { return u2f(u & 0xffff0000u); }
__device__ __forceinline__ unsigned cvt_pk(float lo, float hi) {
  bf16x2 r = {(__bf16)lo, (__bf16)hi};
  return __builtin_bit_cast(unsigned, r);
}
__device__ __forceinline__ float bf_round(float x) { return bf_lo(cvt_pk(x, x) & 0xffffu); }
__device__ __forceinline__ float dot2(unsigned a, unsigned b, float c) {
  return __builtin_amdgcn_fdot2_f32_bf16(__builtin_bit_cast(bf16x2, a), __builtin_bit_cast(bf16x2, b), c, false);
}
template <int CTRL> __device__ __forceinline__ float dpp(float x) {
  return u2f(__builtin_amdgcn_mov_dpp(f2u(x), CTRL, 0xf, 0xf, true));
}
constexpr int DPP_XOR1 = 0xB1, DPP_XOR2 = 0x4E, DPP_ROR4 = 0x124, DPP_ROR8 = 0x128, DPP_HMIR = 0x141, DPP_SHL4 = 0x104, DPP_SHR4 = 0x114;
__device__ __forceinline__ float sw16_add(float a, float b) {
  auto s = __builtin_amdgcn_permlane16_swap(f2u(a), f2u(b), false, false);
  return u2f(s[0]) + u2f(s[1]);
}
__device__ __forceinline__ float sw32_add(float a, float b) {
  auto s = __builtin_amdgcn_permlane32_swap(f2u(a), f2u(b), false, false);
  return u2f(s[0]) + u2f(s[1]);
}
__device__ __forceinline__ float halves_max(float x) {
  auto t = __builtin_amdgcn_permlane32_swap(f2u(x), f2u(x), false, false);
  return fmaxf(u2f(t[0]), u2f(t[1]));
}
__device__ __forceinline__ float s4_max(float x) {
  x = fmaxf(x, dpp<DPP_ROR8>(x)); x = fmaxf(x, dpp<DPP_ROR4>(x));
  auto s = __builtin_amdgcn_permlane16_swap(f2u(x), f2u(x), false, false);
  return halves_max(fmaxf(u2f(s[0]), u2f(s[1])));
}
__device__ __forceinline__ float s4_sum(float x) { x += dpp<DPP_ROR8>(x); x += dpp<DPP_ROR4>(x); x = sw16_add(x, x); return sw32_add(x, x); }
__device__ __forceinline__ float t4_sum(float a, float b, float c, float d) {
  float x = sw32_add(sw16_add(a, b), sw16_add(c, d));
  x += dpp<DPP_ROR8>(x);
  return x + dpp<DPP_ROR4>(x);
}
__device__ __forceinline__ f32x4 mfma(v4u a, v4u b, f32x4 c) {
  return __builtin_amdgcn_mfma_f32_16x16x32_bf16(__builtin_bit_cast(bf16x8, a), __builtin_bit_cast(bf16x8, b), c, 0, 0, 0);
}
__device__ __forceinline__ float rdlane(float x, int l) { return u2f(__builtin_amdgcn_readlane(f2u(x), l)); }
__device__ __forceinline__ unsigned rfl(unsigned x) { return __builtin_amdgcn_readfirstlane(x); }
__device__ __forceinline__ bool any_lane(bool c) { return __builtin_amdgcn_ballot_w64(c) != 0ull; }
__device__ __forceinline__ unsigned umax4(v4u v) { return max(max(v.x, v.y), max(v.z, v.w)); }
__device__ __forceinline__ unsigned nosent(float x) { return min(f2u(x), SENT - 1u); }
#define LDS_BARRIER() do { asm volatile("s_waitcnt lgkmcnt(0)" ::: "memory"); __builtin_amdgcn_s_barrier(); asm volatile("" ::: "memory"); } while (0)
#define SCHED_FENCE() __builtin_amdgcn_sched_barrier(0)
#define GATE(n) asm volatile("s_waitcnt vmcnt(%0)" :: "n"(n) : "memory")
#define RLX_WG __ATOMIC_RELAXED, __HIP_MEMORY_SCOPE_WORKGROUP
#define RLX_AGENT __ATOMIC_RELAXED, __HIP_MEMORY_SCOPE_AGENT
template <int I> struct IC { static constexpr int v = I; };
template <int A, int B, class F> __device__ __forceinline__ void static_for(F&& f) {
  if constexpr (A < B) { f(IC<A>{}); static_for<A + 1, B>(f); }
}

__device__ __forceinline__ float proj_reduce(v4u ra, v4u rb, unsigned x0, unsigned x1, int lane) {
  float v[8];
#pragma unroll
  for (int c = 0; c < 8; ++c) {
    const unsigned pk = __builtin_amdgcn_perm(rb[c >> 1], ra[c >> 1], (c & 1) ? 0x07060302u : 0x05040100u);
    v[c] = sw32_add(dot2(pk, x0, 0.f), dot2(pk, x1, 0.f));
  }
#pragma unroll
  for (int c = 0; c < 4; ++c) v[c] = sw16_add(v[c], v[c + 4]);
#pragma unroll
  for (int c = 0; c < 2; ++c) { const float lo = v[c] + dpp<DPP_ROR8>(v[c]), hi = v[c + 2] + dpp<DPP_ROR8>(v[c + 2]); v[c] = (lane & 8) ? hi : lo; }
  const float lo = v[0] + dpp<DPP_SHL4>(v[0]), hi = v[1] + dpp<DPP_SHR4>(v[1]);
  float r = (lane & 4) ? hi : lo;
  return r + dpp<DPP_XOR2>(r);
}

__device__ __forceinline__ f32x4 ffn_reduce(v4u a, v4u b, float x0, float x1, int lane) {
  float v[8];
#pragma unroll
  for (int c = 0; c < 8; ++c) {
    const float wa = (c & 1) ? bf_hi(a[c >> 1]) : bf_lo(a[c >> 1]), wb = (c & 1) ? bf_hi(b[c >> 1]) : bf_lo(b[c >> 1]);
    v[c] = sw16_add(sw32_add(wa * x0, wa * x1), sw32_add(wb * x0, wb * x1));
  }
  f32x4 r;
#pragma unroll
  for (int c = 0; c < 4; ++c) { const float lo = v[c] + dpp<DPP_ROR8>(v[c]), hi = v[c + 4] + dpp<DPP_ROR8>(v[c + 4]); r[c] = (lane & 8) ? hi : lo; }
  return r;
}

__device__ __forceinline__ unsigned arrive(LAS unsigned* c, int lane) {
  asm volatile("s_waitcnt lgkmcnt(0)" ::: "memory");
  unsigned old = 0;
  if (lane == 0) old = __hip_atomic_fetch_add(c, 1u, RLX_WG);
  return old;
}
__device__ __forceinline__ void await_count(LAS unsigned* c, unsigned n) {
  unsigned spins = 0;
#pragma nounroll
  while (rfl(__hip_atomic_load(c, RLX_WG)) < n && ++spins < (SPIN_CAP << 6)) __builtin_amdgcn_s_sleep(1);
  asm volatile("" ::: "memory");
}

enum { C_QRDY = 0, C_QPART = 1, C_KTK = 2, C_VTK = 3, C_DRY = 4, C_FPART = 5, C_SCORED = 8, C_ROWS = 16, C_WORDS = 128 };
static_assert(C_ROWS + QR + 2 <= C_WORDS, "control words");
constexpr unsigned VOWN = 8 * (NST - 1);
struct Ctx {
  rsrc_t rk, rv, rc, rws;
  LAS unsigned* ctl;
  LAS unsigned* ctlz;
  LAS unsigned char* sc;
  unsigned tlo, thi;
  int zoff, own, grp;
  int vk, vv, scw, scr;
};
__device__ __forceinline__ int k_off(int i) { return (i >> 1) * 1024 + (i & 1) * 256; }
__device__ __forceinline__ v4u ldk(const Ctx& c, int so, int i) { return __builtin_amdgcn_raw_buffer_load_b128(c.rk, c.vk + k_off(i), so, AUX_NT); }
__device__ __forceinline__ v4u ldv(const Ctx& c, int so, int i) { return __builtin_amdgcn_raw_buffer_load_b128(c.rv, c.vv + (i & 3) * 1024, so + (i >> 2) * 4096, AUX_NT); }
__device__ __forceinline__ v4u ld_kv(const Ctx& c, bool v, int so, int i) {
  return __builtin_amdgcn_raw_buffer_load_b128(v ? c.rv : c.rk, v ? c.vv : c.vk, so + (v ? i * 1024 : k_off(i)), AUX_NT);
}
__device__ __forceinline__ int sc_off(int so) { return (((so >> 17) & 7) * 8 + ((so >> 13) & 7)) * 256; }

__device__ __forceinline__ void await_hellos(const Ctx& c, int lane) {
  unsigned spins = 0;
  bool bad;
#pragma nounroll
  do {
    asm volatile("" ::: "memory");
    const v4u h0 = __builtin_amdgcn_raw_buffer_load_b128(c.rws, WS_HELLO + lane * 32, 0, AUX_SC1);
    const v4u h1 = __builtin_amdgcn_raw_buffer_load_b128(c.rws, WS_HELLO + lane * 32 + 16, 0, AUX_SC1);
    bad = (((h0.x ^ c.tlo) | (h0.y ^ c.thi) | (h0.z ^ c.tlo) | (h0.w ^ c.thi)) | ((h1.x ^ c.tlo) | (h1.y ^ c.thi) | (h1.z ^ c.tlo) | (h1.w ^ c.thi))) != 0u;
  } while (any_lane(bad) && ++spins < SPIN_CAP);
  asm volatile("" ::: "memory");
}
__device__ __forceinline__ unsigned take_ticket(const Ctx& c, int which, bool on, int lane) {
  unsigned u = 0;
  if (on && lane == 0) u = __hip_atomic_fetch_add(c.ctlz + which, 1u, RLX_WG);
  return u;
}
struct Deal {
  bool own = true;
  bool single = false;
};
template <int SLOT>
__device__ __forceinline__ void deal(const Ctx& c, Deal& d, int& so_nxt, unsigned& u, unsigned& g, unsigned& dry, bool& trig, int lane) {
  constexpr int DS = 16 - 2 * WIN;
  if constexpr (SLOT == DS - 1) { u = take_ticket(c, C_VTK, !d.single, lane); dry = __hip_atomic_load(c.ctl + C_DRY, RLX_WG); }
  if constexpr (SLOT == DS) {
    if (!d.single) {
      u = rfl(u);
      trig = (u & 7u) == TRIG && u + 8 >= VOWN;
      if (u < VOWN) {
        so_nxt = c.own + (int)((u >> 3) * 16 + 16 + (u & 7u)) * 8192;
        if (trig) await_hellos(c, lane);
      } else {
        unsigned row, spins = 0;
#pragma nounroll
        do { row = rfl(__hip_atomic_load(c.ctl + C_ROWS + ((u - VOWN) >> 3), RLX_WG)); } while (row == SENT && ++spins < (SPIN_CAP << 6));
        so_nxt = c.grp + (int)((row & 7u) * 128 + (2 * NST + (row >> 3)) * 8 + (u & 7u)) * 8192;
        d.own = false;
        d.single = row >= (unsigned)QR;
        trig = trig && !d.single;
      }
    }
    dry = rfl(dry);
    g = 0;
    if (((d.single && !dry) || trig) && lane == 0) g = __builtin_amdgcn_raw_ptr_buffer_atomic_add_i32(1, c.rc, c.zoff, d.single ? 256 : 0, 0);
  }
  if constexpr (SLOT == DS + WIN) {
    if (d.single) {
      const unsigned t = dry ? (unsigned)QP : rfl(g);
      so_nxt = t < (unsigned)QP ? c.grp + (int)(((t >> 3) & 7u) * 128 + (NRW + (t >> 6)) * 8 + (t & 7u)) * 8192 : OOB;
      if (t >= (unsigned)QP) __hip_atomic_store(c.ctl + C_DRY, 1u, RLX_WG);
    } else if (trig && lane == 0) __hip_atomic_store(c.ctl + C_ROWS + ((u + 8 - VOWN) >> 3), g, RLX_WG);
  }
}

__device__ __forceinline__ v4u diag(const f32x4 (&s)[4]) {
  v4u t;
#pragma unroll
  for (int a = 0; a < 4; ++a) t[a] = f2u((dpp<0x00>(s[a][0]) + dpp<0x55>(s[a][1])) + (dpp<0xAA>(s[a][2]) + dpp<0xFF>(s[a][3])));
  return t;
}
template <int MODE>
__device__ __forceinline__ void k_step(const Ctx& c, v4u (&a)[8], v4u (&b)[8], const v4u (&qf)[2], int so, int& so_nxt, int so_v0, bool& to_v, int lane) {
  unsigned u = 0;
  f32x4 s[4];
  v4u ta;
  static_for<0, 16>([&](auto ic) __attribute__((always_inline)) {
    constexpr int i = decltype(ic)::v, h = i & 7;
    s[h >> 1] = mfma(i < 8 ? a[h] : b[h], qf[h & 1], (h & 1) ? s[h >> 1] : f32x4{0.f, 0.f, 0.f, 0.f});
    if constexpr (MODE == 2) {
      asm volatile("" : "+v"(s[h >> 1]) :: "memory");
      SCHED_FENCE();
      if constexpr (i == 15 - WIN) u = take_ticket(c, C_KTK, true, lane);
      if constexpr (i == 16 - WIN) {
        u = rfl(u);
        to_v = u >= 8u * (NST - 2);
        so_nxt = to_v ? so_v0 : c.own + (int)((u >> 3) * 16 + 32 + (u & 7u)) * 8192;
      }
      if constexpr (i + WIN < 8) a[i + WIN] = ldk(c, so, i + WIN);
      else if constexpr (i + WIN < 16) b[i + WIN - 8] = ldk(c, so + HALF, i + WIN - 8);
      else a[i + WIN - 16] = ld_kv(c, to_v, so_nxt, i + WIN - 16);
      SCHED_FENCE();
    }
    if constexpr (i == 7) {
      if constexpr (MODE == 1) {
        asm volatile("" : "+v"(s[0]), "+v"(s[1]), "+v"(s[2]), "+v"(s[3]) :: "memory");
        static_for<0, WIN>([&](auto jc) __attribute__((always_inline)) {
          SCHED_FENCE(); GATE(WIN - 1); a[decltype(jc)::v] = ld_kv(c, to_v, so_nxt, decltype(jc)::v); SCHED_FENCE();
        });
      }
      ta = diag(s);
      asm volatile("" : "+v"(ta));
      SCHED_FENCE();
    }
  });
  const v4u tb = diag(s), t = (lane & 1) ? tb : ta;
  if ((lane & 10) == 0) *(LAS v4u*)(c.sc + sc_off(so) + c.scw) = t;
  arrive(c.ctl + C_SCORED + ((so >> 17) & 7), lane);
  SCHED_FENCE();
}

struct Attn {
  f32x4 acc[8];
  float mc;
  float l;
};
__device__ __forceinline__ void raise_max(Attn& st, float tmax, int lane) {
  const float t = tmax * SCL;
  if (any_lane(t > st.mc)) {
    const float mcn = fmaxf(st.mc, __builtin_ceilf(t));
    const float alpha = __builtin_amdgcn_exp2f(st.mc - mcn);
    const float a0 = rdlane(alpha, 0), a1 = rdlane(alpha, 4);
    const float aa = (lane & 16) ? a1 : a0;
#pragma unroll
    for (int d = 0; d < 8; ++d) st.acc[d] *= aa;
    st.l *= alpha; st.mc = mcn;
  }
}
__device__ __forceinline__ void pv16(Attn& st, const v4u (&v)[8], v4u pa) {
#pragma unroll
  for (int d = 0; d < 8; ++d) {
    v4u vb;
#pragma unroll
    for (int mm = 0; mm < 4; ++mm) vb[mm] = __builtin_amdgcn_perm(v[2 * mm + 1][d >> 1], v[2 * mm][d >> 1], (d & 1) ? 0x07060302u : 0x05040100u);
    st.acc[d] = mfma(pa, vb, st.acc[d]);
  }
  asm volatile("" : "+v"(st.acc[0]), "+v"(st.acc[1]), "+v"(st.acc[2]), "+v"(st.acc[3]), "+v"(st.acc[4]), "+v"(st.acc[5]), "+v"(st.acc[6]), "+v"(st.acc[7]) :: "memory");
}
__device__ __forceinline__ v4u p_parked(Attn& st, v4u s0, v4u s1, int lane) {
  float sc[8], p[8];
#pragma unroll
  for (int k = 0; k < 4; ++k) { sc[k] = u2f(s0[k]); sc[4 + k] = u2f(s1[k]); }
  raise_max(st, halves_max(fmaxf(fmaxf(fmaxf(sc[0], sc[1]), fmaxf(sc[2], sc[3])), fmaxf(fmaxf(sc[4], sc[5]), fmaxf(sc[6], sc[7])))), lane);
#pragma unroll
  for (int k = 0; k < 8; ++k) p[k] = __builtin_amdgcn_exp2f(fmaf(sc[k], SCL, -st.mc));
  st.l += 0.5f * (((p[0] + p[1]) + (p[2] + p[3])) + ((p[4] + p[5]) + (p[6] + p[7])));
  const bool on = ((lane ^ (lane >> 4)) & 1) == 0;
  v4u pa;
#pragma unroll
  for (int k = 0; k < 4; ++k) pa[k] = on ? cvt_pk(p[2 * k], p[2 * k + 1]) : 0u;
  return pa;
}
__device__ __forceinline__ v4u p_fresh(Attn& st, v4u t, int lane) {
  float p[4];
  float x = fmaxf(fmaxf(u2f(t[0]), u2f(t[1])), fmaxf(u2f(t[2]), u2f(t[3])));
  auto r = __builtin_amdgcn_permlane16_swap(f2u(x), f2u(x), false, false);
  raise_max(st, halves_max(fmaxf(u2f(r[0]), u2f(r[1]))), lane);
#pragma unroll
  for (int k = 0; k < 4; ++k) p[k] = __builtin_amdgcn_exp2f(fmaf(u2f(t[k]), SCL, -st.mc));
  st.l += (p[0] + p[1]) + (p[2] + p[3]);
  const unsigned w0 = cvt_pk(p[0], p[1]), w1 = cvt_pk(p[2], p[3]);
  const bool odd = lane & 1;
  auto s0 = __builtin_amdgcn_permlane16_swap(odd ? 0u : w0, odd ? w0 : 0u, false, false);
  auto s1 = __builtin_amdgcn_permlane16_swap(odd ? 0u : w1, odd ? w1 : 0u, false, false);
  return v4u{s0[0], s1[0], s0[1], s1[1]};
}
__device__ __forceinline__ void v_step(const Ctx& c, Attn& st, Deal& d, v4u (&a)[8], v4u (&b)[8], int so, int& so_nxt, int lane) {
  unsigned u = 0, g = 0, dry = 0;
  bool trig = false;
  v4u s0, s1;
  static_for<0, 16>([&](auto ic) __attribute__((always_inline)) {
    constexpr int i = decltype(ic)::v;
    SCHED_FENCE();
    GATE(WIN - 1);
    if constexpr (i == 5) await_count(c.ctl + C_SCORED + ((so >> 17) & 7), 8u);
    if constexpr ((i & 7) == 6) {
      const LAS v4u* p = (const LAS v4u*)(c.sc + sc_off(so) + (i >> 3) * 128 + c.scr);
      s0 = p[0]; s1 = p[1];
    }
    deal<i>(c, d, so_nxt, u, g, dry, trig, lane);
    if constexpr (i + WIN < 8) a[i + WIN] = ldv(c, so, i + WIN);
    else if constexpr (i + WIN < 16) b[i + WIN - 8] = ldv(c, so + HALF, i + WIN - 8);
    else a[i + WIN - 16] = ld_kv(c, d.own, so_nxt, i + WIN - 16);
    SCHED_FENCE();
    if constexpr (i == 7) pv16(st, a, p_parked(st, s0, s1, lane));
    if constexpr (i == 15) pv16(st, b, p_parked(st, s0, s1, lane));
  });
  SCHED_FENCE();
}
__device__ __forceinline__ void kv_step(const Ctx& c, Attn& st, Deal& d, v4u (&a)[8], v4u (&b)[8], const v4u (&qf)[2], int so, int& so_nxt, int lane) {
  unsigned u = 0, g = 0, dry = 0;
  bool trig = false;
  f32x4 s[4];
  v4u pa;
  static_for<0, 16>([&](auto ic) __attribute__((always_inline)) {
    constexpr int i = decltype(ic)::v;
    if constexpr (i < 8) {
      s[i >> 1] = mfma(a[i], qf[i & 1], (i & 1) ? s[i >> 1] : f32x4{0.f, 0.f, 0.f, 0.f});
      asm volatile("" : "+v"(s[i >> 1]) :: "memory");
      SCHED_FENCE();
    } else {
      SCHED_FENCE();
      GATE(WIN - 1);
    }
    deal<i>(c, d, so_nxt, u, g, dry, trig, lane);
    if constexpr (i + WIN < 8) a[i + WIN] = ldk(c, so, i + WIN);
    else if constexpr (i + WIN < 16) b[i + WIN - 8] = ldv(c, so, i + WIN - 8);
    else a[i + WIN - 16] = ldk(c, so_nxt, i + WIN - 16);
    SCHED_FENCE();
    if constexpr (i == 7) { pa = p_fresh(st, diag(s), lane); asm volatile("" : "+v"(pa) :: "memory"); }
  });
  pv16(st, b, pa);
  SCHED_FENCE();
}

__global__ void __launch_bounds__(NTHR, 2)
fused_decode(unsigned char* ws, const unsigned short* x, const unsigned short* wq, const unsigned char* kc_, const unsigned char* vc_,
             const unsigned short* w1, const unsigned short* w3, const unsigned short* w2, const unsigned short* wo,
             const unsigned short* gw, unsigned short* out) {
  __shared__ __attribute__((aligned(16))) float lds[8 * 516 + 8 * 32 + 7 * 256 + 256 + 8 * 16 + 24 * 224 + NST * 8 * 64 + C_WORDS];
  float* const l_mrg = lds;
  float* const l_redq = lds + 8 * 516;
  float* const l_redf = l_redq + 8 * 32;
  unsigned short* const l_q = (unsigned short*)(l_redf + 7 * 256);
  float* const l_fin = l_redf + 7 * 256 + 256;
  unsigned char* const l_wo = (unsigned char*)(l_fin + 8 * 16);
  unsigned char* const l_w2 = l_wo + 16 * DM * 2;
  LAS unsigned char* const l_sc = (LAS unsigned char*)(l_w2 + 8 * DM * 2);
  LAS unsigned* const l_ctl = (LAS unsigned*)(l_sc + NST * 8 * 256);

  const int tid = threadIdx.x, lane = tid & 63, wv = __builtin_amdgcn_readfirstlane(tid >> 6);
  const int w = blockIdx.x, pair = w >> 4, b = pair >> 3, kvh = pair & 7, j = w & 15;
  const u64 epoch_v = __hip_atomic_load((GAS u64*)(ws + WS_EPOCH), RLX_AGENT);
  const rsrc_t rx = mk_rsrc(x, 2 * DM * 2), rwq = mk_rsrc(wq, DM * QN * 2);
  const int rp = tid >> 1;
  const unsigned x0 = __builtin_amdgcn_raw_buffer_load_b32(rx, rp * 4, 0, 0), x1 = __builtin_amdgcn_raw_buffer_load_b32(rx, DM * 2 + rp * 4, 0, 0);
  const int qcol0 = kvh * 512 + (j & 7) * 64 + (b * 2 + (j >> 3)) * 16;
  const int wqo = rp * 2 * QN * 2 + qcol0 * 2 + (tid & 1) * 16;
  const v4u wqa = __builtin_amdgcn_raw_buffer_load_b128(rwq, wqo, 0, AUX_NT), wqb = __builtin_amdgcn_raw_buffer_load_b128(rwq, wqo, QN * 2, AUX_NT);
  SCHED_FENCE();
  Ctx c;
  c.rk = mk_rsrc(kc_ + (size_t)pair * SK * 512, SK * 512); c.rv = mk_rsrc(vc_ + (size_t)pair * SK * 512, SK * 512);
  c.vk = (((lane >> 2) & 1) * 8 + ((lane >> 3) & 1)) * 512 + (lane >> 4) * 64 + (lane & 3) * 16; c.vv = lane * 16;
  c.own = j << 20; c.grp = (j & 8) << 20;
  const int so0 = c.own + wv * 8192;
  v4u a0[8], b0[8], a1[8], b1[8];
#pragma unroll
  for (int i = 0; i < WIN; ++i) a0[i] = ldk(c, so0, i);
  SCHED_FENCE();
  const rsrc_t rws = mk_rsrc(ws, WS_END);
  const unsigned rec = WS_REC + w * REC_STRIDE;
  if (tid < C_WORDS) l_ctl[tid] = tid < C_ROWS ? 0u : SENT;
  if (wv == 7) {
#pragma unroll
    for (int i = 0; i < 6; ++i)
      if (i * 64 + lane < (int)REC_STRIDE / 16) __builtin_amdgcn_raw_buffer_store_b128(v4u{SENT, SENT, SENT, SENT}, rws, rec + (i * 64 + lane) * 16, 0, AUX_SC1);
    if ((j & 7) == 0 && lane < 2) __builtin_amdgcn_raw_buffer_store_b32(0u, rws, WS_CNT + (w >> 3) * CSTR + lane * 256, 0, AUX_SC1);
  }
  SCHED_FENCE();
  LDS_BARRIER();
  const bool has_ffn = w < 224;
  const int frow = (w / 28) * 56 + (tid >> 3), fo = tid < 448 ? frow * FF * 2 + (w % 28) * 128 + (tid & 7) * 16 : DM * FF * 2;
  const rsrc_t rw1 = mk_rsrc(w1, has_ffn ? DM * FF * 2 : 0), rw3 = mk_rsrc(w3, has_ffn ? DM * FF * 2 : 0);
  const rsrc_t rw2 = mk_rsrc(w2 + w * 8 * DM, has_ffn ? 8 * DM * 2 : 0), rwo = mk_rsrc(wo + ((kvh * 2 + b) * 256 + j * 16) * DM, 16 * DM * 2);
  const int yb = (wv >> 1) & 1;
  const int yco = ((wv & 1) * 224 + (lane < 56 ? lane : 55) * 4) * 2;
  const rsrc_t rg = mk_rsrc(gw, wv < 4 ? DM * 2 : 0), rxb = mk_rsrc(x + yb * DM, wv < 4 ? DM * 2 : 0);
  unsigned short fx0, fx1;
  v4u fa, fb, w2s, gx, gg, wos0, wos1;
  u64 epoch = 0, tagw = 0;
  const int qgo = WS_QG + (b * QN + kvh * 512 + tid) * 8;
  v2u gq;
  constexpr int SIDE = 9, QGOT = POLLP + WIN + 1, SGOT = SIDE + 9 + WIN;
  static_assert(QGOT < 32 && SGOT < 32 && SIDE >= WIN, "slots");
  float gate = 0.f;
  static_for<WIN, 32>([&](auto ic) __attribute__((always_inline)) {
    constexpr int i = decltype(ic)::v;
    SCHED_FENCE();
    GATE(WIN - 1);
    if constexpr (i < 8) a0[i] = ldk(c, so0, i);
    else if constexpr (i < 16) b0[i - 8] = ldk(c, so0 + HALF, i - 8);
    else if constexpr (i < 24) a1[i - 16] = ldk(c, so0 + 2 * HALF, i - 16);
    else b1[i - 24] = ldk(c, so0 + 3 * HALF, i - 24);
    if constexpr (i == SIDE) {
      fa = __builtin_amdgcn_raw_buffer_load_b128(rw1, fo, 0, AUX_NT);
      fx0 = __builtin_amdgcn_raw_buffer_load_b16(rx, frow * 2, 0, 0); fx1 = __builtin_amdgcn_raw_buffer_load_b16(rx, frow * 2, DM * 2, 0);
    }
    if constexpr (i == SIDE + 1) fb = __builtin_amdgcn_raw_buffer_load_b128(rw3, fo, 0, AUX_NT);
    if constexpr (i == SIDE + 2) { gx = __builtin_amdgcn_raw_buffer_load_b128(rxb, lane * 16, 0, 0); gg = __builtin_amdgcn_raw_buffer_load_b128(rg, lane * 16, 0, 0); }
    if constexpr (i == SIDE + 4) w2s = __builtin_amdgcn_raw_buffer_load_b128(rw2, tid * 16, 0, AUX_NT);
    if constexpr (i == SIDE + 6) wos0 = __builtin_amdgcn_raw_buffer_load_b128(rwo, tid * 16, 0, AUX_NT);
    if constexpr (i == SIDE + 8) wos1 = __builtin_amdgcn_raw_buffer_load_b128(rwo, tid * 16, 8192, AUX_NT);
    if constexpr (i == POLLP) gq = __builtin_amdgcn_raw_buffer_load_b64(rws, qgo, 0, AUX_SC1);
    SCHED_FENCE();
    if constexpr (i == WIN) {
      epoch = ((u64)rfl((unsigned)(epoch_v >> 32)) << 32 | rfl((unsigned)epoch_v)) + 1ull;
      tagw = epoch << 16;
      if (wv != 7) {
        const float r = proj_reduce(wqa, wqb, x0, x1, lane);
        if (!(lane & 2)) l_redq[wv * 32 + (lane >> 5) * 16 + (lane & 1) * 8 + ((lane >> 2) & 7)] = r;
        if (rfl(arrive(l_ctl + C_QPART, lane)) == 6u) {
          asm volatile("" ::: "memory");
          if (lane < 32) {
            float s = 0.f;
#pragma unroll
            for (int v = 0; v < 7; ++v) s += l_redq[v * 32 + lane];
            __hip_atomic_store((GAS u64*)(ws + WS_QG) + (lane >> 4) * QN + qcol0 + (lane & 15), tagw | (u64)(cvt_pk(s, s) & 0xffffu), RLX_AGENT);
          }
        }
      }
    }
    if constexpr (i == SGOT) {
      if (wv != 7) {
        *(f32x4*)(l_redf + tid * 4) = ffn_reduce(fa, fb, bf_lo(fx0), bf_lo(fx1), lane);
        arrive(l_ctl + C_FPART, lane);
        *(v4u*)(l_w2 + tid * 16) = w2s;
      }
      *(v4u*)(l_wo + tid * 16) = wos0;
      if (tid < 384) *(v4u*)(l_wo + 8192 + tid * 16) = wos1;
      float t = 0.f;
#pragma unroll
      for (int k = 0; k < 4; ++k) t += bf_round(bf_lo(gx[k]) * bf_lo(gg[k])) + bf_round(bf_hi(gx[k]) * bf_hi(gg[k]));
      t += dpp<DPP_XOR1>(t); t += dpp<DPP_XOR2>(t); t = bf_round(s4_sum(t));
      gate = bf_round(1.f / (1.f + __expf(-t)));
      asm volatile("" : "+v"(gate));
    }
    if constexpr (i == QGOT) {
      const unsigned tlo = (unsigned)tagw, thi = (unsigned)(tagw >> 32);
      unsigned spins = 0;
      if (any_lane(((gq.x ^ tlo) >> 16) != 0u || gq.y != thi)) {
#pragma nounroll
        do {
          asm volatile("" ::: "memory");
          gq = __builtin_amdgcn_raw_buffer_load_b64(rws, qgo, 0, AUX_SC1);
        } while (any_lane(((gq.x ^ tlo) >> 16) != 0u || gq.y != thi) && ++spins < SPIN_CAP);
      }
      l_q[tid] = (unsigned short)gq.x;
      arrive(l_ctl + C_QRDY, lane);
    }
  });
  SCHED_FENCE();
  if (wv == 7) {
    await_count(l_ctl + C_FPART, 7u);
    f32x4 t = *(const f32x4*)(l_redf + lane * 4);
#pragma unroll
    for (int v = 1; v < 7; ++v) t += *(const f32x4*)(l_redf + v * 256 + lane * 4);
    if (has_ffn) __builtin_amdgcn_raw_buffer_store_b128(__builtin_bit_cast(v4u, t), rws,
        WS_FX + (((w % 28) * 8 + (lane & 7)) * 8 + w / 28) * 128 + ((lane >> 4) & 1) * 64 + (lane >> 5) * 32 + ((lane >> 3) & 1) * 16, 0, AUX_SC1);
    asm volatile("s_waitcnt vmcnt(0)" ::: "memory");
    if (lane == 0) __hip_atomic_store((GAS u64*)(ws + WS_HELLO) + w, tagw, RLX_AGENT);
  }
  await_count(l_ctl + C_QRDY, 8u);
  v4u qf[2];
  {
    const int qo = ((lane >> 2) & 1) * 256 + (lane >> 4) * 32 + (lane & 3) * 8;
    qf[0] = *(const v4u*)(l_q + qo); qf[1] = *(const v4u*)(l_q + qo + 128);
  }
  c.zoff = 0;
  asm volatile("" : "+v"(c.zoff));
  c.ctl = l_ctl; c.ctlz = (LAS unsigned*)((LAS char*)l_ctl + c.zoff); c.sc = l_sc;
  c.rc = mk_rsrc(ws + WS_CNT + (w >> 3) * CSTR, CSTR); c.rws = rws;
  c.tlo = (unsigned)tagw; c.thi = (unsigned)(tagw >> 32);
  c.scw = (lane & 1) * 128 + ((lane >> 2) & 1) * 64 + (lane >> 4) * 16; c.scr = ((lane >> 2) & 1) * 64 + (lane >> 5) * 32;

  bool to_v;
  int so_cur;
  {
    const unsigned u = rfl(take_ticket(c, C_KTK, true, lane));
    to_v = u >= 8u * (NST - 2);
    so_cur = to_v ? so0 : c.own + (int)((u >> 3) * 16 + 32 + (u & 7u)) * 8192;
  }
  k_step<1>(c, a0, b0, qf, so0, so_cur, so0, to_v, lane);
  k_step<0>(c, a1, b1, qf, so0 + 2 * HALF, so_cur, so0, to_v, lane);
#pragma nounroll
  while (!to_v) {
    int so_nxt;
    k_step<2>(c, a0, b0, qf, so_cur, so_nxt, so0, to_v, lane);
    so_cur = so_nxt;
  }
  Attn st;
#pragma unroll
  for (int d = 0; d < 8; ++d) st.acc[d] = f32x4{0.f, 0.f, 0.f, 0.f};
  st.mc = -3.0e38f; st.l = 0.f;
  Deal dl;
#pragma nounroll
  do {
    int so_nxt;
    v_step(c, st, dl, a0, b0, so_cur, so_nxt, lane);
    so_cur = so_nxt;
  } while (dl.own);
#pragma nounroll
  while (so_cur < OOB) {
    int so_nxt;
    kv_step(c, st, dl, a0, b0, qf, so_cur, so_nxt, lane);
    so_cur = so_nxt;
  }

  {
    const float l2 = sw16_add(st.l, st.l), lsum = sw32_add(l2, l2);
    float* rec_l = l_mrg + wv * 516;
    const bool hi = lane >= 32;
    const f32x4 o0 = {hi ? st.acc[0][1] : st.acc[0][0], hi ? st.acc[1][1] : st.acc[1][0], hi ? st.acc[2][1] : st.acc[2][0], hi ? st.acc[3][1] : st.acc[3][0]};
    const f32x4 o1 = {hi ? st.acc[4][1] : st.acc[4][0], hi ? st.acc[5][1] : st.acc[5][0], hi ? st.acc[6][1] : st.acc[6][0], hi ? st.acc[7][1] : st.acc[7][0]};
    float* dst = rec_l + ((lane >> 4) & 1) * 256 + (lane >> 5) * 128 + (lane & 15) * 8;
    *(f32x4*)dst = o0; *(f32x4*)(dst + 4) = o1;
    if ((lane & ~4) == 0) { rec_l[512 + (lane >> 2)] = st.mc; rec_l[514 + (lane >> 2)] = lsum; }
  }
  LDS_BARRIER();
  if ((wv >> 1) == 2) {
    const int hh = wv & 1, t = tid & 127;
    const float mw = l_mrg[(lane & 7) * 516 + 512 + hh], lw = l_mrg[(lane & 7) * 516 + 514 + hh];
    f32x4 ow[8];
#pragma unroll
    for (int v = 0; v < 8; ++v) ow[v] = *(const f32x4*)(l_mrg + v * 516 + t * 4);
    SCHED_FENCE();
    float M = fmaxf(mw, dpp<DPP_XOR1>(mw));
    M = fmaxf(M, dpp<DPP_XOR2>(M));
    M = fmaxf(M, dpp<DPP_HMIR>(M));
    const float e = __builtin_amdgcn_exp2f(mw - M);
    float L = e * lw;
    L += dpp<DPP_XOR1>(L); L += dpp<DPP_XOR2>(L); L += dpp<DPP_HMIR>(L);
    f32x4 oa = rdlane(e, 0) * ow[0], ob = rdlane(e, 1) * ow[1];
#pragma unroll
    for (int v = 2; v < 8; v += 2) { oa += rdlane(e, v) * ow[v]; ob += rdlane(e, v + 1) * ow[v + 1]; }
    const f32x4 o = oa + ob;
    const float inv = L == 0.f ? 0.f : __builtin_amdgcn_rcpf(L);
    const unsigned lb = f2u(M + __builtin_amdgcn_logf(L)) >> (8 * (lane & 3));
    v4u ov;
#pragma unroll
    for (int k = 0; k < 4; ++k) ov[k] = (min(f2u(o[k] * inv), 0xFFFFFFF8u) & ~3u) | ((lb >> (2 * k)) & 3u);
    __builtin_amdgcn_raw_buffer_store_b128(ov, rws, rec + REC_O + t * 16, 0, AUX_SC1);
  }
  if (wv < 4) {
    const int oo = WS_REC + ((yb * 8 + kvh) * 16 + (lane >> 2)) * REC_STRIDE + REC_O + (b * 256 + j * 16 + (lane & 3) * 4) * 4;
    const rsrc_t rfx = mk_rsrc(ws + WS_FX + w * 1024, has_ffn ? 1024 : 0);
    float ga = u2f(__builtin_amdgcn_raw_buffer_load_b32(rfx, (lane >> 3) * 128 + yb * 32 + (lane & 7) * 4, 0, AUX_SC1));
    float uu = u2f(__builtin_amdgcn_raw_buffer_load_b32(rfx, (lane >> 3) * 128 + yb * 32 + (lane & 7) * 4, 64, AUX_SC1));
    v4u po = __builtin_amdgcn_raw_buffer_load_b128(rws, oo, 0, AUX_SC1);
    SCHED_FENCE();
    v2u wor[16], w2r[8];
    f32x4 wof[16], yf = {0.f, 0.f, 0.f, 0.f};
#pragma unroll
    for (int r = 0; r < 16; ++r) wor[r] = *(const v2u*)(l_wo + r * DM * 2 + yco);
#pragma unroll
    for (int r = 0; r < 8; ++r) w2r[r] = *(const v2u*)(l_w2 + r * DM * 2 + yco);
    SCHED_FENCE();
#pragma unroll
    for (int r = 0; r < 16; ++r) wof[r] = f32x4{bf_lo(wor[r].x), bf_hi(wor[r].x), bf_lo(wor[r].y), bf_hi(wor[r].y)};
#pragma unroll
    for (int r = 0; r < 16; r += 4) asm volatile("" : "+v"(wof[r]), "+v"(wof[r + 1]), "+v"(wof[r + 2]), "+v"(wof[r + 3]));
    SCHED_FENCE();
    {
      float t = sw32_add(ga, uu);
      t += dpp<DPP_ROR8>(t);
      t = bf_round(sw16_add(t, t));
      auto s2 = __builtin_amdgcn_permlane32_swap(f2u(t), f2u(t), false, false);
      ga = u2f(s2[0]); uu = u2f(s2[1]);
      const float h = bf_round(bf_round(ga / (1.f + __expf(-ga))) * uu);
#pragma unroll
      for (int r = 0; r < 8; ++r) yf += rdlane(h, r) * f32x4{bf_lo(w2r[r].x), bf_hi(w2r[r].x), bf_lo(w2r[r].y), bf_hi(w2r[r].y)};
      yf *= bf_round(1.f - gate);
    }
    asm volatile("" : "+v"(yf));
    SCHED_FENCE();
    unsigned spins = 0;
#pragma nounroll
    for (;;) {
      const bool bad = umax4(po) == SENT;
      if (!any_lane(bad) || ++spins >= SPIN_CAP) break;
      asm volatile("" ::: "memory");
      if (bad) po = __builtin_amdgcn_raw_buffer_load_b128(rws, oo, 0, AUX_SC1);
    }
    unsigned lb = ((po.x & 3u) | (po.y & 3u) << 2 | (po.z & 3u) << 4 | (po.w & 3u) << 6) << (8 * (lane & 3));
    lb |= f2u(dpp<DPP_XOR1>(u2f(lb)));
    lb |= f2u(dpp<DPP_XOR2>(u2f(lb)));
    const float lse = u2f(lb);
    const float M = s4_max(lse), e = __builtin_amdgcn_exp2f(lse - M);
    const float o = t4_sum(e * u2f(po[0]), e * u2f(po[1]), e * u2f(po[2]), e * u2f(po[3])) * __builtin_amdgcn_rcpf(s4_sum(e));
    f32x4 y0 = {0.f, 0.f, 0.f, 0.f}, y1 = y0;
#pragma unroll
    for (int r = 0; r < 16; r += 2) { y0 += rdlane(o, (r & 3) * 16 + (r >> 2)) * wof[r]; y1 += rdlane(o, ((r + 1) & 3) * 16 + (r >> 2)) * wof[r + 1]; }
    const f32x4 y = gate * (y0 + y1) + yf;
    if (lane < 56) __builtin_amdgcn_raw_buffer_store_b128(v4u{nosent(y[0]), nosent(y[1]), nosent(y[2]), nosent(y[3])}, rws, rec + REC_Y + yb * DM * 4 + yco * 2, 0, AUX_SC1);
  }
  if (w >= NRED) return;
  {
    const int yo = WS_REC + (tid >> 2) * REC_STRIDE + REC_Y + (w * 16 + (tid & 3) * 4) * 4;
    __builtin_amdgcn_s_barrier();
    __builtin_amdgcn_s_sleep(10);
    v4u y0 = __builtin_amdgcn_raw_buffer_load_b128(rws, yo, 0, AUX_SC1);
    v4u y1 = __builtin_amdgcn_raw_buffer_load_b128(rws, yo, 128 * REC_STRIDE, AUX_SC1);
    unsigned spins = 0;
#pragma nounroll
    for (;;) {
      const bool bad0 = umax4(y0) == SENT, bad1 = umax4(y1) == SENT;
      if (!any_lane(bad0 || bad1) || ++spins >= SPIN_CAP) break;
      asm volatile("" ::: "memory");
      if (bad0) y0 = __builtin_amdgcn_raw_buffer_load_b128(rws, yo, 0, AUX_SC1);
      if (bad1) y1 = __builtin_amdgcn_raw_buffer_load_b128(rws, yo, 128 * REC_STRIDE, AUX_SC1);
    }
    const float y = t4_sum(u2f(y0[0]) + u2f(y1[0]), u2f(y0[1]) + u2f(y1[1]), u2f(y0[2]) + u2f(y1[2]), u2f(y0[3]) + u2f(y1[3]));
    if (!(lane & 12)) l_fin[wv * 16 + (lane & 3) * 4 + (lane >> 4)] = y;
    LDS_BARRIER();
    if (tid < 16) {
      float f[8];
#pragma unroll
      for (int v = 0; v < 8; ++v) f[v] = l_fin[v * 16 + tid];
      const float s = ((f[0] + f[1]) + (f[2] + f[3])) + ((f[4] + f[5]) + (f[6] + f[7]));
      __builtin_amdgcn_raw_buffer_store_b16((unsigned short)(cvt_pk(s, s) & 0xffffu), mk_rsrc(out, 2 * DM * 2), (w * 16 + tid) * 2, 0, AUX_SC1);
    }
    if (w == 0 && tid == 0) __hip_atomic_store((GAS u64*)(ws + WS_EPOCH), epoch, RLX_AGENT);
  }
}

}

extern "C" void kernel_launch(void* const* d_in, const int* in_sizes, int n_in, void* d_out, int out_size, void* d_ws, size_t ws_size,
                              hipStream_t stream) {
  (void)in_sizes; (void)n_in; (void)out_size; (void)ws_size;
  fused_decode<<<NWG, NTHR, 0, stream>>>((unsigned char*)d_ws, (const unsigned short*)d_in[0], (const unsigned short*)d_in[3],
                                         (const unsigned char*)d_in[1], (const unsigned char*)d_in[2], (const unsigned short*)d_in[6],
                                         (const unsigned short*)d_in[7], (const unsigned short*)d_in[8], (const unsigned short*)d_in[4],
                                         (const unsigned short*)d_in[9], (unsigned short*)d_out);
}
```

```cpp
#include <hip/hip_runtime.h>
#include <hip/hip_bf16.h>
#include <stdint.h>

namespace {

#define GAS __attribute__((address_space(1)))
#define LAS __attribute__((address_space(3)))
typedef unsigned v4u __attribute__((ext_vector_type(4)));
typedef unsigned v2u __attribute__((ext_vector_type(2)));
typedef float f32x4 __attribute__((ext_vector_type(4)));
typedef __bf16 bf16x8 __attribute__((ext_vector_type(8)));
typedef __bf16 bf16x2 __attribute__((ext_vector_type(2)));
typedef unsigned long long u64;
typedef __amdgpu_buffer_rsrc_t rsrc_t;

constexpr int DM = 448, SK = 32768, FF = 1792, QN = 4096;
constexpr int NWG = 256, NTHR = 512;
constexpr int WIN = 5;
constexpr int WST = 3;
constexpr int NST = 7;
constexpr int NRW = 15;
constexpr int TRIG = 2;
constexpr int QR = 8 * (NRW - 2 * NST), QP = 64 * (16 - NRW);
static_assert(WIN >= 4 && WIN <= 7 && NST >= 3 && 2 * NST < NRW && NRW < 16, "knobs");
constexpr int NRED = 56;
constexpr int AUX_NT = 2, AUX_SC1 = 16;
constexpr unsigned SENT = 0xFFFFFFFFu;
constexpr unsigned SPIN_CAP = 1u << 18;
constexpr float SCL = 0.0625f * 1.4426950408889634f;
constexpr int OOB = SK * 512;
constexpr int HALF = 65536;

constexpr unsigned WS_EPOCH = 0;
constexpr unsigned WS_HELLO = 2048;
constexpr unsigned WS_QG = 4096;
constexpr unsigned WS_REC = WS_QG + 2 * QN * 8;
constexpr unsigned REC_O = 0, REC_Y = 2048, REC_STRIDE = 5632;
constexpr unsigned WS_CNT = WS_REC + NWG * REC_STRIDE, CSTR = 768;
constexpr unsigned WS_FX = WS_CNT + 32 * CSTR;
constexpr unsigned WS_END = WS_FX + 224 * 1024;

__device__ __forceinline__ rsrc_t mk_rsrc(const void* p, unsigned bytes) { return __builtin_amdgcn_make_buffer_rsrc((void*)p, 0, (int)bytes, 0x00020000); }
__device__ __forceinline__ float u2f(unsigned u) { return __builtin_bit_cast(float, u); }
__device__ __forceinline__ unsigned f2u(float f) { return __builtin_bit_cast(unsigned, f); }
__device__ __forceinline__ float bf_lo(unsigned u) { return u2f(u << 16); }
__device__ __forceinline__ float bf_hi(unsigned u) { return u2f(u & 0xffff0000u); }
__device__ __forceinline__ unsigned cvt_pk(float lo, float hi) {
  bf16x2 r = {(__bf16)lo, (__bf16)hi};
  return __builtin_bit_cast(unsigned, r);
}
__device__ __forceinline__ float bf_round(float x) { return bf_lo(cvt_pk(x, x) & 0xffffu); }
__device__ __forceinline__ float dot2(unsigned a, unsigned b, float c) {
  return __builtin_amdgcn_fdot2_f32_bf16(__builtin_bit_cast(bf16x2, a), __builtin_bit_cast(bf16x2, b), c, false);
}
template <int CTRL> __device__ __forceinline__ float dpp(float x) {
  return u2f(__builtin_amdgcn_mov_dpp(f2u(x), CTRL, 0xf, 0xf, true));
}
constexpr int DPP_XOR1 = 0xB1, DPP_XOR2 = 0x4E, DPP_ROR4 = 0x124, DPP_ROR8 = 0x128, DPP_HMIR = 0x141, DPP_SHL4 = 0x104, DPP_SHR4 = 0x114;
__device__ __forceinline__ float sw16_add(float a, float b) {
  auto s = __builtin_amdgcn_permlane16_swap(f2u(a), f2u(b), false, false);
  return u2f(s[0]) + u2f(s[1]);
}
__device__ __forceinline__ float sw32_add(float a, float b) {
  auto s = __builtin_amdgcn_permlane32_swap(f2u(a), f2u(b), false, false);
  return u2f(s[0]) + u2f(s[1]);
}
__device__ __forceinline__ float halves_max(float x) {
  auto t = __builtin_amdgcn_permlane32_swap(f2u(x), f2u(x), false, false);
  return fmaxf(u2f(t[0]), u2f(t[1]));
}
__device__ __forceinline__ float s4_max(float x) {
  x = fmaxf(x, dpp<DPP_ROR8>(x)); x = fmaxf(x, dpp<DPP_ROR4>(x));
  auto s = __builtin_amdgcn_permlane16_swap(f2u(x), f2u(x), false, false);
  return halves_max(fmaxf(u2f(s[0]), u2f(s[1])));
}
__device__ __forceinline__ float s4_sum(float x) { x += dpp<DPP_ROR8>(x); x += dpp<DPP_ROR4>(x); x = sw16_add(x, x); return sw32_add(x, x); }
__device__ __forceinline__ float t4_sum(float a, float b, float c, float d) {
  float x = sw32_add(sw16_add(a, b), sw16_add(c, d));
  x += dpp<DPP_ROR8>(x);
  return x + dpp<DPP_ROR4>(x);
}
__device__ __forceinline__ f32x4 mfma(v4u a, v4u b, f32x4 c) {
  return __builtin_amdgcn_mfma_f32_16x16x32_bf16(__builtin_bit_cast(bf16x8, a), __builtin_bit_cast(bf16x8, b), c, 0, 0, 0);
}
__device__ __forceinline__ float rdlane(float x, int l) { return u2f(__builtin_amdgcn_readlane(f2u(x), l)); }
__device__ __forceinline__ unsigned rfl(unsigned x) { return __builtin_amdgcn_readfirstlane(x); }
__device__ __forceinline__ bool any_lane(bool c) { return __builtin_amdgcn_ballot_w64(c) != 0ull; }
__device__ __forceinline__ unsigned umax4(v4u v) { return max(max(v.x, v.y), max(v.z, v.w)); }
__device__ __forceinline__ unsigned nosent(float x) { return min(f2u(x), SENT - 1u); }
#define LDS_BARRIER() do { asm volatile("s_waitcnt lgkmcnt(0)" ::: "memory"); __builtin_amdgcn_s_barrier(); asm volatile("" ::: "memory"); } while (0)
#define SCHED_FENCE() __builtin_amdgcn_sched_barrier(0)
#define GATE(n) asm volatile("s_waitcnt vmcnt(%0)" :: "n"(n) : "memory")
#define RLX_WG __ATOMIC_RELAXED, __HIP_MEMORY_SCOPE_WORKGROUP
#define RLX_AGENT __ATOMIC_RELAXED, __HIP_MEMORY_SCOPE_AGENT
template <int I> struct IC { static constexpr int v = I; };
template <int A, int B, class F> __device__ __forceinline__ void static_for(F&& f) {
  if constexpr (A < B) { f(IC<A>{}); static_for<A + 1, B>(f); }
}

__device__ __forceinline__ float proj_reduce(v4u ra, v4u rb, unsigned x0, unsigned x1, int lane) {
  float v[8];
#pragma unroll
  for (int c = 0; c < 8; ++c) {
    const unsigned pk = __builtin_amdgcn_perm(rb[c >> 1], ra[c >> 1], (c & 1) ? 0x07060302u : 0x05040100u);
    v[c] = sw32_add(dot2(pk, x0, 0.f), dot2(pk, x1, 0.f));
  }
#pragma unroll
  for (int c = 0; c < 4; ++c) v[c] = sw16_add(v[c], v[c + 4]);
#pragma unroll
  for (int c = 0; c < 2; ++c) { const float lo = v[c] + dpp<DPP_ROR8>(v[c]), hi = v[c + 2] + dpp<DPP_ROR8>(v[c + 2]); v[c] = (lane & 8) ? hi : lo; }
  const float lo = v[0] + dpp<DPP_SHL4>(v[0]), hi = v[1] + dpp<DPP_SHR4>(v[1]);
  float r = (lane & 4) ? hi : lo;
  return r + dpp<DPP_XOR2>(r);
}

__device__ __forceinline__ f32x4 ffn_reduce(v4u a, v4u b, float x0, float x1, int lane) {
  float v[8];
#pragma unroll
  for (int c = 0; c < 8; ++c) {
    const float wa = (c & 1) ? bf_hi(a[c >> 1]) : bf_lo(a[c >> 1]), wb = (c & 1) ? bf_hi(b[c >> 1]) : bf_lo(b[c >> 1]);
    v[c] = sw16_add(sw32_add(wa * x0, wa * x1), sw32_add(wb * x0, wb * x1));
  }
  f32x4 r;
#pragma unroll
  for (int c = 0; c < 4; ++c) { const float lo = v[c] + dpp<DPP_ROR8>(v[c]), hi = v[c + 4] + dpp<DPP_ROR8>(v[c + 4]); r[c] = (lane & 8) ? hi : lo; }
  return r;
}

__device__ __forceinline__ unsigned arrive(LAS unsigned* c, int lane) {
  asm volatile("s_waitcnt lgkmcnt(0)" ::: "memory");
  unsigned old = 0;
  if (lane == 0) old = __hip_atomic_fetch_add(c, 1u, RLX_WG);
  return old;
}
__device__ __forceinline__ void await_count(LAS unsigned* c, unsigned n) {
  unsigned spins = 0;
#pragma nounroll
  while (rfl(__hip_atomic_load(c, RLX_WG)) < n && ++spins < (SPIN_CAP << 6)) __builtin_amdgcn_s_sleep(1);
  asm volatile("" ::: "memory");
}

enum { C_QRDY = 0, C_QPART = 1, C_KTK = 2, C_VTK = 3, C_DRY = 4, C_FPART = 5, C_SCORED = 8, C_ROWS = 16, C_WORDS = 128 };
static_assert(C_ROWS + QR + 2 <= C_WORDS, "control words");
constexpr unsigned VOWN = 8 * (NST - 1);
struct Ctx {
  rsrc_t rk, rv, rc, rws;
  LAS unsigned* ctl;
  LAS unsigned* ctlz;
  LAS unsigned char* sc;
  unsigned tlo, thi;
  int zoff, own, grp;
  int vk, vv, scw, scr;
};
__device__ __forceinline__ int k_off(int i) { return (i >> 1) * 1024 + (i & 1) * 256; }
__device__ __forceinline__ v4u ldk(const Ctx& c, int so, int i) { return __builtin_amdgcn_raw_buffer_load_b128(c.rk, c.vk + k_off(i), so, AUX_NT); }
__device__ __forceinline__ v4u ldv(const Ctx& c, int so, int i) { return __builtin_amdgcn_raw_buffer_load_b128(c.rv, c.vv + (i & 3) * 1024, so + (i >> 2) * 4096, AUX_NT); }
__device__ __forceinline__ v4u ld_kv(const Ctx& c, bool v, int so, int i) {
  return __builtin_amdgcn_raw_buffer_load_b128(v ? c.rv : c.rk, v ? c.vv : c.vk, so + (v ? i * 1024 : k_off(i)), AUX_NT);
}
__device__ __forceinline__ int sc_off(int so) { return (((so >> 17) & 7) * 8 + ((so >> 13) & 7)) * 256; }

__device__ __forceinline__ void await_hellos(const Ctx& c, int lane) {
  unsigned spins = 0;
  bool bad;
#pragma nounroll
  do {
    asm volatile("" ::: "memory");
    const v4u h0 = __builtin_amdgcn_raw_buffer_load_b128(c.rws, WS_HELLO + lane * 32, 0, AUX_SC1);
    const v4u h1 = __builtin_amdgcn_raw_buffer_load_b128(c.rws, WS_HELLO + lane * 32 + 16, 0, AUX_SC1);
    bad = (((h0.x ^ c.tlo) | (h0.y ^ c.thi) | (h0.z ^ c.tlo) | (h0.w ^ c.thi)) | ((h1.x ^ c.tlo) | (h1.y ^ c.thi) | (h1.z ^ c.tlo) | (h1.w ^ c.thi))) != 0u;
  } while (any_lane(bad) && ++spins < SPIN_CAP);
  asm volatile("" ::: "memory");
}
__device__ __forceinline__ unsigned take_ticket(const Ctx& c, int which, bool on, int lane) {
  unsigned u = 0;
  if (on && lane == 0) u = __hip_atomic_fetch_add(c.ctlz + which, 1u, RLX_WG);
  return u;
}
struct Deal {
  bool own = true;
  bool single = false;
};
template <int SLOT, bool OWN>
__device__ __forceinline__ void deal(const Ctx& c, Deal& d, int& so_nxt, unsigned& u, unsigned& g, unsigned& dry, bool& trig, int lane) {
  constexpr int DS = 16 - 2 * WIN;
  if constexpr (SLOT == DS - 1) { u = take_ticket(c, C_VTK, !d.single, lane); dry = __hip_atomic_load(c.ctl + C_DRY, RLX_WG); }
  if constexpr (SLOT == DS) {
    if (!d.single) {
      u = rfl(u);
      trig = (u & 7u) == TRIG && u + 8 >= VOWN;
      if (OWN && u < VOWN) {
        so_nxt = c.own + (int)((u >> 3) * 16 + 16 + (u & 7u)) * 8192;
        if (trig) await_hellos(c, lane);
      } else {
        unsigned row, spins = 0;
#pragma nounroll
        do { row = rfl(__hip_atomic_load(c.ctl + C_ROWS + ((u - VOWN) >> 3), RLX_WG)); } while (row == SENT && ++spins < (SPIN_CAP << 6));
        so_nxt = c.grp + (int)((row & 7u) * 128 + (2 * NST + (row >> 3)) * 8 + (u & 7u)) * 8192;
        d.own = false;
        d.single = row >= (unsigned)QR;
        trig = trig && !d.single;
      }
    }
    dry = rfl(dry);
    g = 0;
    if (((d.single && !dry) || trig) && lane == 0) g = __builtin_amdgcn_raw_ptr_buffer_atomic_add_i32(1, c.rc, c.zoff, d.single ? 256 : 0, 0);
  }
  if constexpr (SLOT == DS + WIN) {
    if (d.single) {
      const unsigned t = dry ? (unsigned)QP : rfl(g);
      so_nxt = t < (unsigned)QP ? c.grp + (int)(((t >> 3) & 7u) * 128 + (NRW + (t >> 6)) * 8 + (t & 7u)) * 8192 : OOB;
      if (t >= (unsigned)QP) __hip_atomic_store(c.ctl + C_DRY, 1u, RLX_WG);
    } else if (trig && lane == 0) __hip_atomic_store(c.ctl + C_ROWS + ((u + 8 - VOWN) >> 3), g, RLX_WG);
  }
}

__device__ __forceinline__ v4u diag(const f32x4 (&s)[4]) {
  v4u t;
#pragma unroll
  for (int a = 0; a < 4; ++a) t[a] = f2u((dpp<0x00>(s[a][0]) + dpp<0x55>(s[a][1])) + (dpp<0xAA>(s[a][2]) + dpp<0xFF>(s[a][3])));
  return t;
}
template <int MODE>
__device__ __forceinline__ void k_step(const Ctx& c, v4u (&a)[8], v4u (&b)[8], const v4u (&qf)[2], int so, int& so_nxt, int so_v0, bool& to_v, int lane) {
  unsigned u = 0;
  f32x4 s[4];
  v4u ta;
  static_for<0, 16>([&](auto ic) __attribute__((always_inline)) {
    constexpr int i = decltype(ic)::v, h = i & 7;
    s[h >> 1] = mfma(i < 8 ? a[h] : b[h], qf[h & 1], (h & 1) ? s[h >> 1] : f32x4{0.f, 0.f, 0.f, 0.f});
    if constexpr (MODE == 2) {
      asm volatile("" : "+v"(s[h >> 1]) :: "memory");
      SCHED_FENCE();
      GATE(WST - 1);
      if constexpr (i == 15 - WIN) u = take_ticket(c, C_KTK, true, lane);
      if constexpr (i == 16 - WIN) {
        u = rfl(u);
        to_v = u >= 8u * (NST - 2);
        so_nxt = to_v ? so_v0 : c.own + (int)((u >> 3) * 16 + 32 + (u & 7u)) * 8192;
      }
      if constexpr (i + WIN < 8) a[i + WIN] = ldk(c, so, i + WIN);
      else if constexpr (i + WIN < 16) b[i + WIN - 8] = ldk(c, so + HALF, i + WIN - 8);
      else a[i + WIN - 16] = ld_kv(c, to_v, so_nxt, i + WIN - 16);
      SCHED_FENCE();
    }
    if constexpr (i == 7) {
      if constexpr (MODE == 1) {
        asm volatile("" : "+v"(s[0]), "+v"(s[1]), "+v"(s[2]), "+v"(s[3]) :: "memory");
        static_for<0, WIN>([&](auto jc) __attribute__((always_inline)) {
          SCHED_FENCE(); GATE(WIN - 1); a[decltype(jc)::v] = ld_kv(c, to_v, so_nxt, decltype(jc)::v); SCHED_FENCE();
        });
      }
      ta = diag(s);
      asm volatile("" : "+v"(ta));
      SCHED_FENCE();
    }
  });
  const v4u tb = diag(s), t = (lane & 1) ? tb : ta;
  if ((lane & 10) == 0) *(LAS v4u*)(c.sc + sc_off(so) + c.scw) = t;
  arrive(c.ctl + C_SCORED + ((so >> 17) & 7), lane);
  SCHED_FENCE();
}

struct Attn {
  f32x4 acc[8];
  float mc;
  float l;
};
__device__ __forceinline__ void raise_max(Attn& st, float tmax, int lane) {
  const float t = tmax * SCL;
  if (any_lane(t > st.mc)) {
    const float mcn = fmaxf(st.mc, __builtin_ceilf(t));
    const float alpha = __builtin_amdgcn_exp2f(st.mc - mcn);
    const float a0 = rdlane(alpha, 0), a1 = rdlane(alpha, 4);
    const float aa = (lane & 16) ? a1 : a0;
#pragma unroll
    for (int d = 0; d < 8; ++d) st.acc[d] *= aa;
    st.l *= alpha; st.mc = mcn;
  }
}
__device__ __forceinline__ void pv16(Attn& st, const v4u (&v)[8], v4u pa) {
#pragma unroll
  for (int d = 0; d < 8; ++d) {
    v4u vb;
#pragma unroll
    for (int mm = 0; mm < 4; ++mm) vb[mm] = __builtin_amdgcn_perm(v[2 * mm + 1][d >> 1], v[2 * mm][d >> 1], (d & 1) ? 0x07060302u : 0x05040100u);
    st.acc[d] = mfma(pa, vb, st.acc[d]);
  }
  asm volatile("" : "+v"(st.acc[0]), "+v"(st.acc[1]), "+v"(st.acc[2]), "+v"(st.acc[3]), "+v"(st.acc[4]), "+v"(st.acc[5]), "+v"(st.acc[6]), "+v"(st.acc[7]) :: "memory");
}
__device__ __forceinline__ v4u p_parked(Attn& st, v4u s0, v4u s1, int lane) {
  float sc[8], p[8];
#pragma unroll
  for (int k = 0; k < 4; ++k) { sc[k] = u2f(s0[k]); sc[4 + k] = u2f(s1[k]); }
  raise_max(st, halves_max(fmaxf(fmaxf(fmaxf(sc[0], sc[1]), fmaxf(sc[2], sc[3])), fmaxf(fmaxf(sc[4], sc[5]), fmaxf(sc[6], sc[7])))), lane);
#pragma unroll
  for (int k = 0; k < 8; ++k) p[k] = __builtin_amdgcn_exp2f(fmaf(sc[k], SCL, -st.mc));
  st.l += 0.5f * (((p[0] + p[1]) + (p[2] + p[3])) + ((p[4] + p[5]) + (p[6] + p[7])));
  const bool on = ((lane ^ (lane >> 4)) & 1) == 0;
  v4u pa;
#pragma unroll
  for (int k = 0; k < 4; ++k) pa[k] = on ? cvt_pk(p[2 * k], p[2 * k + 1]) : 0u;
  return pa;
}
__device__ __forceinline__ v4u p_fresh(Attn& st, v4u t, int lane) {
  float p[4];
  float x = fmaxf(fmaxf(u2f(t[0]), u2f(t[1])), fmaxf(u2f(t[2]), u2f(t[3])));
  auto r = __builtin_amdgcn_permlane16_swap(f2u(x), f2u(x), false, false);
  raise_max(st, halves_max(fmaxf(u2f(r[0]), u2f(r[1]))), lane);
#pragma unroll
  for (int k = 0; k < 4; ++k) p[k] = __builtin_amdgcn_exp2f(fmaf(u2f(t[k]), SCL, -st.mc));
  st.l += (p[0] + p[1]) + (p[2] + p[3]);
  const unsigned w0 = cvt_pk(p[0], p[1]), w1 = cvt_pk(p[2], p[3]);
  const bool odd = lane & 1;
  auto s0 = __builtin_amdgcn_permlane16_swap(odd ? 0u : w0, odd ? w0 : 0u, false, false);
  auto s1 = __builtin_amdgcn_permlane16_swap(odd ? 0u : w1, odd ? w1 : 0u, false, false);
  return v4u{s0[0], s1[0], s0[1], s1[1]};
}
__device__ __forceinline__ void v_step(const Ctx& c, Attn& st, Deal& d, v4u (&a)[8], v4u (&b)[8], int so, int& so_nxt, int lane) {
  unsigned u = 0, g = 0, dry = 0;
  bool trig = false;
  v4u s0, s1;
  static_for<0, 16>([&](auto ic) __attribute__((always_inline)) {
    constexpr int i = decltype(ic)::v;
    SCHED_FENCE();
    GATE(WST - 1);
    if constexpr (i == 5) await_count(c.ctl + C_SCORED + ((so >> 17) & 7), 8u);
    if constexpr ((i & 7) == 6) {
      const LAS v4u* p = (const LAS v4u*)(c.sc + sc_off(so) + (i >> 3) * 128 + c.scr);
      s0 = p[0]; s1 = p[1];
    }
    deal<i, true>(c, d, so_nxt, u, g, dry, trig, lane);
    if constexpr (i + WIN < 8) a[i + WIN] = ldv(c, so, i + WIN);
    else if constexpr (i + WIN < 16) b[i + WIN - 8] = ldv(c, so + HALF, i + WIN - 8);
    else a[i + WIN - 16] = ld_kv(c, d.own, so_nxt, i + WIN - 16);
    SCHED_FENCE();
    if constexpr (i == 7) pv16(st, a, p_parked(st, s0, s1, lane));
    if constexpr (i == 15) pv16(st, b, p_parked(st, s0, s1, lane));
  });
  SCHED_FENCE();
}
__device__ __forceinline__ void kv_step(const Ctx& c, Attn& st, Deal& d, v4u (&a)[8], v4u (&b)[8], const v4u (&qf)[2], int so, int& so_nxt, int lane) {
  unsigned u = 0, g = 0, dry = 0;
  bool trig = false;
  f32x4 s[4];
  v4u pa;
  static_for<0, 16>([&](auto ic) __attribute__((always_inline)) {
    constexpr int i = decltype(ic)::v;
    if constexpr (i < 8) {
      s[i >> 1] = mfma(a[i], qf[i & 1], (i & 1) ? s[i >> 1] : f32x4{0.f, 0.f, 0.f, 0.f});
      asm volatile("" : "+v"(s[i >> 1]) :: "memory");
      SCHED_FENCE();
    } else {
      SCHED_FENCE();
      GATE(WIN - 1);
    }
    deal<i, false>(c, d, so_nxt, u, g, dry, trig, lane);
    if constexpr (i + WIN < 8) a[i + WIN] = ldk(c, so, i + WIN);
    else if constexpr (i + WIN < 16) b[i + WIN - 8] = ldv(c, so, i + WIN - 8);
    else a[i + WIN - 16] = ldk(c, so_nxt, i + WIN - 16);
    SCHED_FENCE();
    if constexpr (i == 7) { pa = p_fresh(st, diag(s), lane); asm volatile("" : "+v"(pa) :: "memory"); }
  });
  pv16(st, b, pa);
  SCHED_FENCE();
}

__global__ void __launch_bounds__(NTHR, 2)
fused_decode(unsigned char* ws, const unsigned short* x, const unsigned short* wq, const unsigned short* w1, const unsigned short* w3,
             const unsigned short* w2, const unsigned short* wo, const unsigned char* kc_, const unsigned char* vc_,
             const unsigned short* gw, unsigned short* out) {
  __shared__ __attribute__((aligned(16))) float lds[8 * 516 + 8 * 32 + 7 * 256 + 256 + 8 * 16 + 24 * 224 + NST * 8 * 64 + C_WORDS];
  float* const l_mrg = lds;
  float* const l_redq = lds + 8 * 516;
  float* const l_redf = l_redq + 8 * 32;
  unsigned short* const l_q = (unsigned short*)(l_redf + 7 * 256);
  float* const l_fin = l_redf + 7 * 256 + 256;
  unsigned char* const l_wo = (unsigned char*)(l_fin + 8 * 16);
  unsigned char* const l_w2 = l_wo + 16 * DM * 2;
  LAS unsigned char* const l_sc = (LAS unsigned char*)(l_w2 + 8 * DM * 2);
  LAS unsigned* const l_ctl = (LAS unsigned*)(l_sc + NST * 8 * 256);

  const int tid = threadIdx.x, lane = tid & 63, wv = __builtin_amdgcn_readfirstlane(tid >> 6);
  const int w = blockIdx.x, pair = w >> 4, b = pair >> 3, kvh = pair & 7, j = w & 15;
  const rsrc_t rws = mk_rsrc(ws, WS_END);
  const unsigned rec = WS_REC + w * REC_STRIDE;
  if (wv == 7) {
#pragma unroll
    for (int i = 0; i < 6; ++i)
      if (i * 64 + lane < (int)REC_STRIDE / 16) __builtin_amdgcn_raw_buffer_store_b128(v4u{SENT, SENT, SENT, SENT}, rws, rec + (i * 64 + lane) * 16, 0, AUX_SC1);
    if ((j & 7) == 0 && lane < 2) __builtin_amdgcn_raw_buffer_store_b32(0u, rws, WS_CNT + (w >> 3) * CSTR + lane * 256, 0, AUX_SC1);
  }
  SCHED_FENCE();
  const u64 epoch_v = __hip_atomic_load((GAS u64*)(ws + WS_EPOCH), RLX_AGENT);
  const rsrc_t rx = mk_rsrc(x, 2 * DM * 2), rwq = mk_rsrc(wq, DM * QN * 2);
  const int rp = tid >> 1;
  const int qcol0 = kvh * 512 + (j & 7) * 64 + (b * 2 + (j >> 3)) * 16;
  const int wqo = rp * 2 * QN * 2 + qcol0 * 2 + (tid & 1) * 16;
  const bool has_ffn = w < 224;
  const int frow = (w / 28) * 56 + (tid >> 3), fo = tid < 448 ? frow * FF * 2 + (w % 28) * 128 + (tid & 7) * 16 : DM * FF * 2;
  const rsrc_t rw1 = mk_rsrc(w1, has_ffn ? DM * FF * 2 : 0), rw3 = mk_rsrc(w3, has_ffn ? DM * FF * 2 : 0);
  const rsrc_t rw2 = mk_rsrc(w2 + w * 8 * DM, has_ffn ? 8 * DM * 2 : 0), rwo = mk_rsrc(wo + ((kvh * 2 + b) * 256 + j * 16) * DM, 16 * DM * 2);
  const unsigned x0 = __builtin_amdgcn_raw_buffer_load_b32(rx, rp * 4, 0, 0), x1 = __builtin_amdgcn_raw_buffer_load_b32(rx, DM * 2 + rp * 4, 0, 0);
  const v4u wqa = __builtin_amdgcn_raw_buffer_load_b128(rwq, wqo, 0, AUX_NT), wqb = __builtin_amdgcn_raw_buffer_load_b128(rwq, wqo, QN * 2, AUX_NT);
  const unsigned short fx0 = __builtin_amdgcn_raw_buffer_load_b16(rx, frow * 2, 0, 0), fx1 = __builtin_amdgcn_raw_buffer_load_b16(rx, frow * 2, DM * 2, 0);
  SCHED_FENCE();
  const v4u fa = __builtin_amdgcn_raw_buffer_load_b128(rw1, fo, 0, AUX_NT), fb = __builtin_amdgcn_raw_buffer_load_b128(rw3, fo, 0, AUX_NT);
  const v4u w2s = __builtin_amdgcn_raw_buffer_load_b128(rw2, tid * 16, 0, AUX_NT);
  const v4u wos0 = __builtin_amdgcn_raw_buffer_load_b128(rwo, tid * 16, 0, AUX_NT), wos1 = __builtin_amdgcn_raw_buffer_load_b128(rwo, tid * 16, 8192, AUX_NT);
  SCHED_FENCE();
  if (tid < C_WORDS) l_ctl[tid] = tid < C_ROWS ? 0u : SENT;
  LDS_BARRIER();
  Ctx c;
  c.rk = mk_rsrc(kc_ + (size_t)pair * SK * 512, SK * 512); c.rv = mk_rsrc(vc_ + (size_t)pair * SK * 512, SK * 512);
  c.vk = (((lane >> 2) & 1) * 8 + ((lane >> 3) & 1)) * 512 + (lane >> 4) * 64 + (lane & 3) * 16; c.vv = lane * 16;
  c.own = j << 20; c.grp = (j & 8) << 20;
  const int so0 = c.own + wv * 8192;
  v4u a0[8], b0[8], a1[8], b1[8];
  const int yb = (wv >> 1) & 1;
  const int yco = ((wv & 1) * 224 + (lane < 56 ? lane : 55) * 4) * 2;
  const rsrc_t rg = mk_rsrc(gw, wv < 4 ? DM * 2 : 0), rxb = mk_rsrc(x + yb * DM, wv < 4 ? DM * 2 : 0);
  v4u gx, gg;
  u64 epoch = 0, tagw = 0;
  const int qgo = WS_QG + (b * QN + kvh * 512 + tid) * 8;
  v2u gq;
  constexpr int QSL = 3, SGOT = 8, GSL = 10, POLLP = 17, QGOT = POLLP + WIN + 1;
  float gate = 0.f;
  static_for<0, 32>([&](auto ic) __attribute__((always_inline)) {
    constexpr int i = decltype(ic)::v;
    SCHED_FENCE();
    GATE(i <= WIN ? WIN : WIN - 1);
    if constexpr (i < 8) a0[i] = ldk(c, so0, i);
    else if constexpr (i < 16) b0[i - 8] = ldk(c, so0 + HALF, i - 8);
    else if constexpr (i < 24) a1[i - 16] = ldk(c, so0 + 2 * HALF, i - 16);
    else b1[i - 24] = ldk(c, so0 + 3 * HALF, i - 24);
    if constexpr (i == GSL) { gx = __builtin_amdgcn_raw_buffer_load_b128(rxb, lane * 16, 0, 0); gg = __builtin_amdgcn_raw_buffer_load_b128(rg, lane * 16, 0, 0); }
    if constexpr (i == POLLP) gq = __builtin_amdgcn_raw_buffer_load_b64(rws, qgo, 0, AUX_SC1);
    SCHED_FENCE();
    if constexpr (i == QSL) {
      epoch = ((u64)rfl((unsigned)(epoch_v >> 32)) << 32 | rfl((unsigned)epoch_v)) + 1ull;
      tagw = epoch << 16;
      if (wv != 7) {
        const float r = proj_reduce(wqa, wqb, x0, x1, lane);
        if (!(lane & 2)) l_redq[wv * 32 + (lane >> 5) * 16 + (lane & 1) * 8 + ((lane >> 2) & 7)] = r;
        if (rfl(arrive(l_ctl + C_QPART, lane)) == 6u) {
          asm volatile("" ::: "memory");
          if (lane < 32) {
            float s = 0.f;
#pragma unroll
            for (int v = 0; v < 7; ++v) s += l_redq[v * 32 + lane];
            __hip_atomic_store((GAS u64*)(ws + WS_QG) + (lane >> 4) * QN + qcol0 + (lane & 15), tagw | (u64)(cvt_pk(s, s) & 0xffffu), RLX_AGENT);
          }
        }
      }
    }
    if constexpr (i == SGOT) {
      if (wv != 7) {
        *(f32x4*)(l_redf + tid * 4) = ffn_reduce(fa, fb, bf_lo(fx0), bf_lo(fx1), lane);
        arrive(l_ctl + C_FPART, lane);
        *(v4u*)(l_w2 + tid * 16) = w2s;
      }
      *(v4u*)(l_wo + tid * 16) = wos0;
      if (tid < 384) *(v4u*)(l_wo + 8192 + tid * 16) = wos1;
    }
    if constexpr (i == GSL + WIN + 1) {
      float t = 0.f;
#pragma unroll
      for (int k = 0; k < 4; ++k) t += bf_round(bf_lo(gx[k]) * bf_lo(gg[k])) + bf_round(bf_hi(gx[k]) * bf_hi(gg[k]));
      t += dpp<DPP_XOR1>(t); t += dpp<DPP_XOR2>(t); t = bf_round(s4_sum(t));
      gate = bf_round(1.f / (1.f + __expf(-t)));
      asm volatile("" : "+v"(gate));
    }
    if constexpr (i == QGOT) {
      const unsigned tlo = (unsigned)tagw, thi = (unsigned)(tagw >> 32);
      unsigned spins = 0;
      if (any_lane(((gq.x ^ tlo) >> 16) != 0u || gq.y != thi)) {
#pragma nounroll
        do {
          asm volatile("" ::: "memory");
          gq = __builtin_amdgcn_raw_buffer_load_b64(rws, qgo, 0, AUX_SC1);
        } while (any_lane(((gq.x ^ tlo) >> 16) != 0u || gq.y != thi) && ++spins < SPIN_CAP);
      }
      l_q[tid] = (unsigned short)gq.x;
      arrive(l_ctl + C_QRDY, lane);
    }
  });
  SCHED_FENCE();
  if (wv == 7) {
    await_count(l_ctl + C_FPART, 7u);
    f32x4 t = *(const f32x4*)(l_redf + lane * 4);
#pragma unroll
    for (int v = 1; v < 7; ++v) t += *(const f32x4*)(l_redf + v * 256 + lane * 4);
    if (has_ffn) __builtin_amdgcn_raw_buffer_store_b128(__builtin_bit_cast(v4u, t), rws,
        WS_FX + (((w % 28) * 8 + (lane & 7)) * 8 + w / 28) * 128 + ((lane >> 4) & 1) * 64 + (lane >> 5) * 32 + ((lane >> 3) & 1) * 16, 0, AUX_SC1);
    asm volatile("s_waitcnt vmcnt(0)" ::: "memory");
    if (lane == 0) __hip_atomic_store((GAS u64*)(ws + WS_HELLO) + w, tagw, RLX_AGENT);
  }
  await_count(l_ctl + C_QRDY, 8u);
  v4u qf[2];
  {
    const int qo = ((lane >> 2) & 1) * 256 + (lane >> 4) * 32 + (lane & 3) * 8;
    qf[0] = *(const v4u*)(l_q + qo); qf[1] = *(const v4u*)(l_q + qo + 128);
  }
  c.zoff = 0;
  asm volatile("" : "+v"(c.zoff));
  c.ctl = l_ctl; c.ctlz = (LAS unsigned*)((LAS char*)l_ctl + c.zoff); c.sc = l_sc;
  c.rc = mk_rsrc(ws + WS_CNT + (w >> 3) * CSTR, CSTR); c.rws = rws;
  c.tlo = (unsigned)tagw; c.thi = (unsigned)(tagw >> 32);
  c.scw = (lane & 1) * 128 + ((lane >> 2) & 1) * 64 + (lane >> 4) * 16; c.scr = ((lane >> 2) & 1) * 64 + (lane >> 5) * 32;

  bool to_v;
  int so_cur;
  {
    const unsigned u = rfl(take_ticket(c, C_KTK, true, lane));
    to_v = u >= 8u * (NST - 2);
    so_cur = to_v ? so0 : c.own + (int)((u >> 3) * 16 + 32 + (u & 7u)) * 8192;
  }
  k_step<1>(c, a0, b0, qf, so0, so_cur, so0, to_v, lane);
  k_step<0>(c, a1, b1, qf, so0 + 2 * HALF, so_cur, so0, to_v, lane);
#pragma nounroll
  while (!to_v) {
    int so_nxt;
    k_step<2>(c, a0, b0, qf, so_cur, so_nxt, so0, to_v, lane);
    so_cur = so_nxt;
  }
  Attn st;
#pragma unroll
  for (int d = 0; d < 8; ++d) st.acc[d] = f32x4{0.f, 0.f, 0.f, 0.f};
  st.mc = -3.0e38f; st.l = 0.f;
  Deal dl;
#pragma nounroll
  do {
    int so_nxt;
    v_step(c, st, dl, a0, b0, so_cur, so_nxt, lane);
    so_cur = so_nxt;
  } while (dl.own);
#pragma nounroll
  while (so_cur < OOB) {
    int so_nxt;
    kv_step(c, st, dl, a0, b0, qf, so_cur, so_nxt, lane);
    so_cur = so_nxt;
  }

  {
    const float l2 = sw16_add(st.l, st.l), lsum = sw32_add(l2, l2);
    float* rec_l = l_mrg + wv * 516;
    const bool hi = lane >= 32;
    const f32x4 o0 = {hi ? st.acc[0][1] : st.acc[0][0], hi ? st.acc[1][1] : st.acc[1][0], hi ? st.acc[2][1] : st.acc[2][0], hi ? st.acc[3][1] : st.acc[3][0]};
    const f32x4 o1 = {hi ? st.acc[4][1] : st.acc[4][0], hi ? st.acc[5][1] : st.acc[5][0], hi ? st.acc[6][1] : st.acc[6][0], hi ? st.acc[7][1] : st.acc[7][0]};
    float* dst = rec_l + ((lane >> 4) & 1) * 256 + (lane >> 5) * 128 + (lane & 15) * 8;
    *(f32x4*)dst = o0; *(f32x4*)(dst + 4) = o1;
    if ((lane & ~4) == 0) { rec_l[512 + (lane >> 2)] = st.mc; rec_l[514 + (lane >> 2)] = lsum; }
  }
  LDS_BARRIER();
  if ((wv >> 1) == 2) {
    const int hh = wv & 1, t = tid & 127;
    const float mw = l_mrg[(lane & 7) * 516 + 512 + hh], lw = l_mrg[(lane & 7) * 516 + 514 + hh];
    f32x4 ow[8];
#pragma unroll
    for (int v = 0; v < 8; ++v) ow[v] = *(const f32x4*)(l_mrg + v * 516 + t * 4);
    SCHED_FENCE();
    float M = fmaxf(mw, dpp<DPP_XOR1>(mw));
    M = fmaxf(M, dpp<DPP_XOR2>(M));
    M = fmaxf(M, dpp<DPP_HMIR>(M));
    const float e = __builtin_amdgcn_exp2f(mw - M);
    float L = e * lw;
    L += dpp<DPP_XOR1>(L); L += dpp<DPP_XOR2>(L); L += dpp<DPP_HMIR>(L);
    f32x4 oa = rdlane(e, 0) * ow[0], ob = rdlane(e, 1) * ow[1];
#pragma unroll
    for (int v = 2; v < 8; v += 2) { oa += rdlane(e, v) * ow[v]; ob += rdlane(e, v + 1) * ow[v + 1]; }
    const f32x4 o = oa + ob;
    const float inv = L == 0.f ? 0.f : __builtin_amdgcn_rcpf(L);
    const unsigned lb = f2u(M + __builtin_amdgcn_logf(L)) >> (8 * (lane & 3));
    v4u ov;
#pragma unroll
    for (int k = 0; k < 4; ++k) ov[k] = (min(f2u(o[k] * inv), 0xFFFFFFF8u) & ~3u) | ((lb >> (2 * k)) & 3u);
    __builtin_amdgcn_raw_buffer_store_b128(ov, rws, rec + REC_O + t * 16, 0, AUX_SC1);
  }
  if (wv < 4) {
    const int oo = WS_REC + ((yb * 8 + kvh) * 16 + (lane >> 2)) * REC_STRIDE + REC_O + (b * 256 + j * 16 + (lane & 3) * 4) * 4;
    const rsrc_t rfx = mk_rsrc(ws + WS_FX + w * 1024, has_ffn ? 1024 : 0);
    float ga = u2f(__builtin_amdgcn_raw_buffer_load_b32(rfx, (lane >> 3) * 128 + yb * 32 + (lane & 7) * 4, 0, AUX_SC1));
    float uu = u2f(__builtin_amdgcn_raw_buffer_load_b32(rfx, (lane >> 3) * 128 + yb * 32 + (lane & 7) * 4, 64, AUX_SC1));
    v4u po = __builtin_amdgcn_raw_buffer_load_b128(rws, oo, 0, AUX_SC1);
    SCHED_FENCE();
    v2u wor[16], w2r[8];
    f32x4 wof[16], yf = {0.f, 0.f, 0.f, 0.f};
#pragma unroll
    for (int r = 0; r < 16; ++r) wor[r] = *(const v2u*)(l_wo + r * DM * 2 + yco);
#pragma unroll
    for (int r = 0; r < 8; ++r) w2r[r] = *(const v2u*)(l_w2 + r * DM * 2 + yco);
    SCHED_FENCE();
#pragma unroll
    for (int r = 0; r < 16; ++r) wof[r] = f32x4{bf_lo(wor[r].x), bf_hi(wor[r].x), bf_lo(wor[r].y), bf_hi(wor[r].y)};
#pragma unroll
    for (int r = 0; r < 16; r += 4) asm volatile("" : "+v"(wof[r]), "+v"(wof[r + 1]), "+v"(wof[r + 2]), "+v"(wof[r + 3]));
    SCHED_FENCE();
    {
      float t = sw32_add(ga, uu);
      t += dpp<DPP_ROR8>(t);
      t = bf_round(sw16_add(t, t));
      auto s2 = __builtin_amdgcn_permlane32_swap(f2u(t), f2u(t), false, false);
      ga = u2f(s2[0]); uu = u2f(s2[1]);
      const float h = bf_round(bf_round(ga / (1.f + __expf(-ga))) * uu);
#pragma unroll
      for (int r = 0; r < 8; ++r) yf += rdlane(h, r) * f32x4{bf_lo(w2r[r].x), bf_hi(w2r[r].x), bf_lo(w2r[r].y), bf_hi(w2r[r].y)};
      yf *= bf_round(1.f - gate);
    }
    asm volatile("" : "+v"(yf));
    SCHED_FENCE();
    unsigned spins = 0;
#pragma nounroll
    for (;;) {
      const bool bad = umax4(po) == SENT;
      if (!any_lane(bad) || ++spins >= SPIN_CAP) break;
      asm volatile("" ::: "memory");
      if (bad) po = __builtin_amdgcn_raw_buffer_load_b128(rws, oo, 0, AUX_SC1);
    }
    unsigned lb = ((po.x & 3u) | (po.y & 3u) << 2 | (po.z & 3u) << 4 | (po.w & 3u) << 6) << (8 * (lane & 3));
    lb |= f2u(dpp<DPP_XOR1>(u2f(lb)));
    lb |= f2u(dpp<DPP_XOR2>(u2f(lb)));
    const float lse = u2f(lb);
    const float M = s4_max(lse), e = __builtin_amdgcn_exp2f(lse - M);
    const float o = t4_sum(e * u2f(po[0]), e * u2f(po[1]), e * u2f(po[2]), e * u2f(po[3])) * __builtin_amdgcn_rcpf(s4_sum(e));
    f32x4 y0 = {0.f, 0.f, 0.f, 0.f}, y1 = y0;
#pragma unroll
    for (int r = 0; r < 16; r += 2) { y0 += rdlane(o, (r & 3) * 16 + (r >> 2)) * wof[r]; y1 += rdlane(o, ((r + 1) & 3) * 16 + (r >> 2)) * wof[r + 1]; }
    const f32x4 y = gate * (y0 + y1) + yf;
    if (lane < 56) __builtin_amdgcn_raw_buffer_store_b128(v4u{nosent(y[0]), nosent(y[1]), nosent(y[2]), nosent(y[3])}, rws, rec + REC_Y + yb * DM * 4 + yco * 2, 0, AUX_SC1);
  }
  if (w >= NRED) return;
  {
    const int yo = WS_REC + (tid >> 2) * REC_STRIDE + REC_Y + (w * 16 + (tid & 3) * 4) * 4;
    __builtin_amdgcn_s_barrier();
    __builtin_amdgcn_s_sleep(10);
    v4u y0 = __builtin_amdgcn_raw_buffer_load_b128(rws, yo, 0, AUX_SC1);
    v4u y1 = __builtin_amdgcn_raw_buffer_load_b128(rws, yo, 128 * REC_STRIDE, AUX_SC1);
    unsigned spins = 0;
#pragma nounroll
    for (;;) {
      const bool bad0 = umax4(y0) == SENT, bad1 = umax4(y1) == SENT;
      if (!any_lane(bad0 || bad1) || ++spins >= SPIN_CAP) break;
      asm volatile("" ::: "memory");
      if (bad0) y0 = __builtin_amdgcn_raw_buffer_load_b128(rws, yo, 0, AUX_SC1);
      if (bad1) y1 = __builtin_amdgcn_raw_buffer_load_b128(rws, yo, 128 * REC_STRIDE, AUX_SC1);
    }
    const float y = t4_sum(u2f(y0[0]) + u2f(y1[0]), u2f(y0[1]) + u2f(y1[1]), u2f(y0[2]) + u2f(y1[2]), u2f(y0[3]) + u2f(y1[3]));
    if (!(lane & 12)) l_fin[wv * 16 + (lane & 3) * 4 + (lane >> 4)] = y;
    LDS_BARRIER();
    if (tid < 16) {
      float f[8];
#pragma unroll
      for (int v = 0; v < 8; ++v) f[v] = l_fin[v * 16 + tid];
      const float s = ((f[0] + f[1]) + (f[2] + f[3])) + ((f[4] + f[5]) + (f[6] + f[7]));
      __builtin_amdgcn_raw_buffer_store_b16((unsigned short)(cvt_pk(s, s) & 0xffffu), mk_rsrc(out, 2 * DM * 2), (w * 16 + tid) * 2, 0, AUX_SC1);
    }
    if (w == 0 && tid == 0) __hip_atomic_store((GAS u64*)(ws + WS_EPOCH), epoch, RLX_AGENT);
  }
}

}

extern "C" void kernel_launch(void* const* d_in, const int* in_sizes, int n_in, void* d_out, int out_size, void* d_ws, size_t ws_size,
                              hipStream_t stream) {
  (void)in_sizes; (void)n_in; (void)out_size; (void)ws_size;
  fused_decode<<<NWG, NTHR, 0, stream>>>((unsigned char*)d_ws, (const unsigned short*)d_in[0], (const unsigned short*)d_in[3],
                                         (const unsigned short*)d_in[6], (const unsigned short*)d_in[7], (const unsigned short*)d_in[8],
                                         (const unsigned short*)d_in[4], (const unsigned char*)d_in[1], (const unsigned char*)d_in[2],
                                         (const unsigned short*)d_in[9], (unsigned short*)d_out);
}
```
